```python
import torch

def _softmax(x, axis=-1):
    return torch.softmax(x, dim=axis)
def _gelu(x):
    return torch.nn.functional.gelu(x, approximate="tanh")
def _silu(x):
    return torch.nn.functional.silu(x)
def _sigmoid(x):
    return torch.sigmoid(x)
def _layernorm(x, axis=-1, eps=1e-5):
    m = x.mean(dim=axis, keepdim=True)
    v = x.var(dim=axis, keepdim=True, unbiased=False)
    return (x - m) / torch.sqrt(v + eps)
def _scatter_add(tgt, idx, src, axis=0):
    return tgt.clone().index_add_(axis, idx, src)
def _segment_sum(src, seg, n):
    out = torch.zeros((n,) + src.shape[1:], dtype=src.dtype)
    return out.index_add_(0, seg, src)
def _pad(x, pw):
    flat = tuple(v for pair in reversed(pw) for v in pair)
    return torch.nn.functional.pad(x, flat)

kernel_name = 'recurrent_scan__linear_attn_block__glu_gated_glu__prompt'

def setup_inputs():

    _w = (lambda a: (lambda t: t.to(torch.bfloat16) if t.is_floating_point() else t)(torch.as_tensor(a)))
    def _A_setup():
        B, S, D = 1, 2048, 640
        return {
            "x": _w((lambda *s: torch.randn(*s, dtype=torch.bfloat16) * 0.05)(B, S, D)),
            "wqkv": _w((lambda *s: torch.randn(*s, dtype=torch.bfloat16) * 0.05)(D, 3 * D)),
            "wo": _w((lambda *s: torch.randn(*s, dtype=torch.bfloat16) * 0.05)(D, D)),
        }
    _A_in = _A_setup()

    def _B_setup():
        B, S, D, F = 1, 2048, 640, 1728
        return {
            "x": _w((lambda *s: torch.randn(*s, dtype=torch.bfloat16) * 0.05)(B, S, D)),
            "w_gate": _w((lambda *s: torch.randn(*s, dtype=torch.bfloat16) * 0.05)(D, F)),
            "w_up": _w((lambda *s: torch.randn(*s, dtype=torch.bfloat16) * 0.05)(D, F)),
            "w_down": _w((lambda *s: torch.randn(*s, dtype=torch.bfloat16) * 0.05)(F, D)),
        }
    _B_in = _B_setup()

    return {
        "A_x": _A_in["x"],
        "A_wqkv": _A_in["wqkv"],
        "A_wo": _A_in["wo"],
        "B_x": _B_in["x"],
        "B_w_gate": _B_in["w_gate"],
        "B_w_up": _B_in["w_up"],
        "B_w_down": _B_in["w_down"],
    }

def reference(A_x, A_wqkv, A_wo, B_x, B_w_gate, B_w_up, B_w_down):
    def _A_ref(x, wqkv, wo):
        B, S, D = x.shape
        H = 32; Dh = D // H
        qkv = (lambda x, *d: x.permute(*d))((x @ wqkv).reshape(B, S, 3, H, Dh), 2, 0, 3, 1, 4)
        q, k, v = qkv[0], qkv[1], qkv[2]
        phi = lambda t: torch.where(t > 0, t + 1, torch.exp(t)) + 1e-6
        pq, pk = phi(q), phi(k)
        kv = torch.einsum("bhsd,bhse->bhde", pk, v)
        z = torch.einsum("bhsd->bhd", pk)
        num = torch.einsum("bhqd,bhde->bhqe", pq, kv)
        den = torch.einsum("bhqd,bhd->bhq", pq, z)[..., None] + 1e-6
        o = num / den
        return (lambda x, *d: x.permute(*d))(o, 0, 2, 1, 3).reshape(B, S, D) @ wo

    def _B_ref(x, w_gate, w_up, w_down):
        g = torch.einsum("bsd,df->bsf", x, w_gate)
        u = torch.einsum("bsd,df->bsf", x, w_up)
        h = _sigmoid(g) * u
        return torch.einsum("bsf,fd->bsd", h, w_down)

    x = A_x
    xn = _layernorm(x, axis=-1)
    s = _A_ref(xn, A_wqkv, A_wo)
    g = _silu(_B_ref(xn, B_w_gate, B_w_up, B_w_down))
    return x + s * g
```

```cpp
#include <hip/hip_runtime.h>
#include <hip/hip_bf16.h>
#include <stdint.h>

typedef unsigned short u16;
typedef __attribute__((ext_vector_type(8))) short bf16x8;
typedef __attribute__((ext_vector_type(4))) short s16x4;
typedef __attribute__((ext_vector_type(4))) float f32x4;
typedef __attribute__((ext_vector_type(2))) __bf16 bf2;
typedef __attribute__((ext_vector_type(4))) unsigned u32x4;
typedef __attribute__((ext_vector_type(2))) unsigned u32x2;
#define LAS __attribute__((address_space(3)))

constexpr int S_ = 2048, D_ = 640, F_ = 1728, NH = 32, DH = 20;
constexpr int KVSZ = DH * DH + DH;

__device__ __forceinline__ unsigned pack2(float a, float b) {
  bf2 v = {(__bf16)a, (__bf16)b};
  return __builtin_bit_cast(unsigned, v);
}
__device__ __forceinline__ float rbf(float a) { return (float)(__bf16)a; }
__device__ __forceinline__ float bflo(unsigned u) { return __uint_as_float(u << 16); }
__device__ __forceinline__ float bfhi(unsigned u) { return __uint_as_float(u & 0xffff0000u); }
__device__ __forceinline__ float dot_ones(unsigned a, float c) {
  bf2 one = {(__bf16)1.0f, (__bf16)1.0f};
  return __builtin_amdgcn_fdot2_f32_bf16(__builtin_bit_cast(bf2, a), one, c, false);
}
__device__ __forceinline__ float dot_self(unsigned a, float c) {
  bf2 v = __builtin_bit_cast(bf2, a);
  return __builtin_amdgcn_fdot2_f32_bf16(v, v, c, false);
}
__device__ __forceinline__ void glds16(const void* g, void* l) {
  __builtin_amdgcn_global_load_lds((const unsigned*)g, (unsigned*)l, 16, 0, 0);
}
#define WAIT_V(n) asm volatile("s_waitcnt vmcnt(" #n ")" ::: "memory")
#define BAR() __builtin_amdgcn_s_barrier()
__device__ __forceinline__ bf16x8 tr_frag(const char* p0, const char* p1) {
  s16x4 a = __builtin_amdgcn_ds_read_tr16_b64_v4i16((LAS s16x4*)p0);
  s16x4 b = __builtin_amdgcn_ds_read_tr16_b64_v4i16((LAS s16x4*)p1);
  bf16x8 r;
  r[0] = a[0]; r[1] = a[1]; r[2] = a[2]; r[3] = a[3];
  r[4] = b[0]; r[5] = b[1]; r[6] = b[2]; r[7] = b[3];
  return r;
}
__device__ __forceinline__ float phi_bf(float t) {
  float p = t > 0.f ? rbf(t + 1.0f) : rbf(__expf(t));
  return rbf(p + 1e-6f);
}

constexpr int K1_ABYTES = 256 * 64 * 2;
constexpr int K1_BBYTES = 64 * 192 * 2;
constexpr int K1_STAGE = K1_ABYTES + K1_BBYTES;
constexpr int K1_OFF_RS = 2 * K1_STAGE;
constexpr int K1_OFF_CS = K1_OFF_RS + 4096;
constexpr int K1_OFF_MEAN = K1_OFF_CS + 6144;
constexpr int K1_OFF_CSF = K1_OFF_MEAN + 2048;
constexpr int K1_LDS = K1_OFF_CSF + 768;

template <int ROLE>
__device__ __forceinline__ void k1_body(const u16* __restrict__ x, const u16* __restrict__ w0,
                                        const u16* __restrict__ w1, u16* __restrict__ outp,
                                        float* __restrict__ kvp, int rb, int cgl, char* smem) {
  constexpr int BN = ROLE == 2 ? 192 : 160;
  constexpr int NB = BN / 16;
  constexpr int WM = ROLE == 1 ? 8 : 4;
  constexpr int WN = 8 / WM;
  constexpr int MT = 16 / WM;
  constexpr int NT = NB / WN;
  constexpr int BPIECES = 64 * BN * 2 / 1024;
  constexpr int LDW = ROLE == 2 ? F_ : 3 * D_;
  constexpr int HALFN = BN / 2;

  const int tid = threadIdx.x;
  const int wid = __builtin_amdgcn_readfirstlane(tid >> 6);
  const int lane = tid & 63;
  const int g = lane >> 4, l15 = lane & 15;
  const int wm = wid % WM, wn = wid / WM;
  const int m0 = rb * 256;

  const u16* asrc = x + (size_t)(m0 + wid * 8 + (lane >> 3)) * D_ + (((lane & 7) ^ (lane >> 3)) * 8);
  const u16* bsrc[3];
#pragma unroll
  for (int i = 0; i < 3; ++i) {
    int p = wid + 8 * i;
    if (p >= BPIECES) p = wid;
    int c = p * 64 + lane;
    int sub = c >> 4, kb = sub / NB, nb = sub % NB;
    int prow = (c >> 1) & 7, half = c & 1;
    int row = prow ^ ((kb & 1) << 2);
    int k = 8 * kb + row, n = 16 * nb + 8 * half;
    const u16* wp;
    if (ROLE == 0) wp = w0 + cgl * 160 + n;
    else if (ROLE == 1) wp = (n < HALFN) ? (w0 + D_ + cgl * 80 + n) : (w0 + 2 * D_ + cgl * 80 + n - HALFN);
    else wp = (n < HALFN) ? (w0 + cgl * 96 + n) : (w1 + cgl * 96 + n - HALFN);
    bsrc[i] = wp + (size_t)k * LDW;
  }
  auto stage = [&](int t, int buf) {
    char* sa = smem + buf * K1_STAGE;
    char* sb = sa + K1_ABYTES;
#pragma unroll
    for (int i = 0; i < 4; ++i)
      glds16(asrc + (size_t)i * 64 * D_ + t * 64, sa + (wid + 8 * i) * 1024);
#pragma unroll
    for (int i = 0; i < 3; ++i)
      if (wid + 8 * i < BPIECES) glds16(bsrc[i] + (size_t)t * 64 * LDW, sb + (wid + 8 * i) * 1024);
  };

  const int arow = wm * (MT * 16) + l15;
  const int aoff0 = arow * 128 + ((g ^ (lane & 7)) * 16);
  const int bq = l15 >> 2, bp = lane & 3;
  const int boffc = g * NB * 256 + bq * 32 + bp * 8;
  const int boff0 = boffc + 128 * (g & 1), boff1 = boffc + 128 * ((g & 1) ^ 1);

  f32x4 acc[MT][NT];
#pragma unroll
  for (int i = 0; i < MT; ++i)
#pragma unroll
    for (int j = 0; j < NT; ++j) acc[i][j] = (f32x4){0.f, 0.f, 0.f, 0.f};
  float rs[MT], rq[MT], cs[NT];
#pragma unroll
  for (int i = 0; i < MT; ++i) { rs[i] = 0.f; rq[i] = 0.f; }
#pragma unroll
  for (int j = 0; j < NT; ++j) cs[j] = 0.f;

  stage(0, 0);
  for (int t = 0; t < 10; ++t) {
    WAIT_V(0);
    BAR();
    if (t + 1 < 10) stage(t + 1, (t + 1) & 1);
    const char* sa = smem + (t & 1) * K1_STAGE;
    const char* sb = sa + K1_ABYTES;
    const bool do_cs = (t % WM) == wm;
#pragma unroll
    for (int s = 0; s < 2; ++s) {
      bf16x8 xf[MT], wf[NT];
#pragma unroll
      for (int i = 0; i < MT; ++i) xf[i] = *(const bf16x8*)(sa + ((aoff0 ^ (s * 64)) + i * 2048));
#pragma unroll
      for (int j = 0; j < NT; ++j) {
        int nb;
        if (ROLE == 2) nb = (j < 3) ? (wn * 3 + j) : (6 + wn * 3 + (j - 3));
        else nb = wn * NT + j;
        nb = __builtin_amdgcn_readfirstlane(nb);
        wf[j] = tr_frag(sb + boff0 + (4 * s * NB + nb) * 256, sb + boff1 + (4 * s * NB + nb) * 256);
      }
#pragma unroll
      for (int i = 0; i < MT; ++i)
#pragma unroll
        for (int j = 0; j < NT; ++j) {
          if (ROLE == 1) acc[i][j] = __builtin_amdgcn_mfma_f32_16x16x32_bf16(xf[i], wf[j], acc[i][j], 0, 0, 0);
          else acc[i][j] = __builtin_amdgcn_mfma_f32_16x16x32_bf16(wf[j], xf[i], acc[i][j], 0, 0, 0);
        }
      if (WN == 1 || s == wn) {
#pragma unroll
        for (int i = 0; i < MT; ++i) {
          u32x4 u = __builtin_bit_cast(u32x4, xf[i]);
#pragma unroll
          for (int e = 0; e < 4; ++e) { rs[i] = dot_ones(u[e], rs[i]); rq[i] = dot_self(u[e], rq[i]); }
        }
      }
      if (do_cs) {
#pragma unroll
        for (int j = 0; j < NT; ++j) {
          u32x4 u = __builtin_bit_cast(u32x4, wf[j]);
#pragma unroll
          for (int e = 0; e < 4; ++e) cs[j] = dot_ones(u[e], cs[j]);
        }
      }
    }
  }

  float* rsum = (float*)(smem + K1_OFF_RS);
  float* csum = (float*)(smem + K1_OFF_CS);
  float* smean = (float*)(smem + K1_OFF_MEAN);
  float* srsd = smean + 256;
  float* scs = (float*)(smem + K1_OFF_CSF);
#pragma unroll
  for (int i = 0; i < MT; ++i) {
    float a = rs[i], b = rq[i];
    a += __shfl_xor(a, 16); b += __shfl_xor(b, 16);
    a += __shfl_xor(a, 32); b += __shfl_xor(b, 32);
    if (g == 0) {
      int r = wm * (MT * 16) + i * 16 + l15;
      rsum[(wn * 256 + r) * 2] = a;
      rsum[(wn * 256 + r) * 2 + 1] = b;
    }
  }
#pragma unroll
  for (int j = 0; j < NT; ++j) {
    float a = cs[j];
    a += __shfl_xor(a, 16);
    a += __shfl_xor(a, 32);
    int nb;
    if (ROLE == 2) nb = (j < 3) ? (wn * 3 + j) : (6 + wn * 3 + (j - 3));
    else nb = wn * NT + j;
    if (g == 0) csum[wm * 192 + nb * 16 + l15] = a;
  }
  __syncthreads();
  if (tid < 256) {
    float a = rsum[tid * 2], b = rsum[tid * 2 + 1];
    if (WN == 2) { a += rsum[(256 + tid) * 2]; b += rsum[(256 + tid) * 2 + 1]; }
    float mean = a * (1.0f / D_);
    float var = fmaxf(b * (1.0f / D_) - mean * mean, 0.f);
    float sd = rbf(sqrtf(rbf(rbf(var) + 1e-5f)));
    smean[tid] = rbf(mean);
    srsd[tid] = 1.0f / sd;
  } else if (tid - 256 < BN) {
    int n = tid - 256;
    float a = 0.f;
#pragma unroll
    for (int w = 0; w < WM; ++w) a += csum[w * 192 + n];
    scs[n] = a;
  }
  __syncthreads();

  if (ROLE == 0) {
#pragma unroll
    for (int i = 0; i < MT; ++i) {
      int r = wm * 64 + i * 16 + l15;
      float mean = smean[r], rsd = srsd[r];
#pragma unroll
      for (int j = 0; j < NT; ++j) {
        int n = (wn * NT + j) * 16 + 4 * g;
        f32x4 c4 = *(const f32x4*)(scs + n);
        float p[4];
#pragma unroll
        for (int e = 0; e < 4; ++e) p[e] = phi_bf(rbf((acc[i][j][e] - mean * c4[e]) * rsd));
        u32x2 o = {pack2(p[0], p[1]), pack2(p[2], p[3])};
        *(u32x2*)(outp + (size_t)(m0 + r) * D_ + cgl * 160 + n) = o;
      }
    }
  } else if (ROLE == 2) {
#pragma unroll
    for (int i = 0; i < MT; ++i) {
      int r = wm * 64 + i * 16 + l15;
      float mean = smean[r], rsd = srsd[r];
#pragma unroll
      for (int j = 0; j < 3; ++j) {
        int n = (wn * 3 + j) * 16 + 4 * g;
        f32x4 cg4 = *(const f32x4*)(scs + n);
        f32x4 cu4 = *(const f32x4*)(scs + 96 + n);
        float hv[4];
#pragma unroll
        for (int e = 0; e < 4; ++e) {
          float gv = rbf((acc[i][j][e] - mean * cg4[e]) * rsd);
          float uv = rbf((acc[i][j + 3][e] - mean * cu4[e]) * rsd);
          float sg = rbf(1.0f / (1.0f + __expf(-gv)));
          hv[e] = sg * uv;
        }
        u32x2 o = {pack2(hv[0], hv[1]), pack2(hv[2], hv[3])};
        *(u32x2*)(outp + (size_t)(m0 + r) * F_ + cgl * 96 + n) = o;
      }
    }
  } else {
    bf16x8 fr[10];
    float mean[2][4], rsd[2][4];
#pragma unroll
    for (int i = 0; i < 2; ++i)
#pragma unroll
      for (int e = 0; e < 4; ++e) {
        int r = wid * 32 + i * 16 + 4 * g + e;
        mean[i][e] = smean[r];
        rsd[i][e] = srsd[r];
      }
#pragma unroll
    for (int j = 0; j < 10; ++j) {
      float c = scs[j * 16 + l15];
      float v[8];
#pragma unroll
      for (int i = 0; i < 2; ++i)
#pragma unroll
        for (int e = 0; e < 4; ++e) {
          float t = rbf((acc[i][j][e] - mean[i][e] * c) * rsd[i][e]);
          v[i * 4 + e] = (j < 5) ? phi_bf(t) : t;
        }
      u32x4 u = {pack2(v[0], v[1]), pack2(v[2], v[3]), pack2(v[4], v[5]), pack2(v[6], v[7])};
      fr[j] = __builtin_bit_cast(bf16x8, u);
    }
    float* red = (float*)smem;
    float* myred = red + wid * (4 * KVSZ);
    const bf16x8 ones = {0x3F80, 0x3F80, 0x3F80, 0x3F80, 0x3F80, 0x3F80, 0x3F80, 0x3F80};
#pragma unroll
    for (int i = 0; i < 5; ++i) {
#pragma unroll
      for (int j = 0; j < 5; ++j) {
        if (j < i - 1 || j > i + 1) continue;
        f32x4 z4 = {0.f, 0.f, 0.f, 0.f};
        f32x4 d = __builtin_amdgcn_mfma_f32_16x16x32_bf16(fr[i], fr[5 + j], z4, 0, 0, 0);
        int vc = 16 * j + l15, hv = vc / DH, ev = vc - hv * DH;
#pragma unroll
        for (int e = 0; e < 4; ++e) {
          int kc = 16 * i + 4 * g + e, hk = kc / DH, dk = kc - hk * DH;
          if (hk == hv) myred[hk * KVSZ + dk * DH + ev] = d[e];
        }
      }
      f32x4 z4 = {0.f, 0.f, 0.f, 0.f};
      f32x4 d = __builtin_amdgcn_mfma_f32_16x16x32_bf16(fr[i], ones, z4, 0, 0, 0);
      if (l15 == 0) {
#pragma unroll
        for (int e = 0; e < 4; ++e) {
          int kc = 16 * i + 4 * g + e, hk = kc / DH, dk = kc - hk * DH;
          myred[hk * KVSZ + DH * DH + dk] = d[e];
        }
      }
    }
    __syncthreads();
    for (int o = tid; o < 4 * KVSZ; o += 512) {
      float a = 0.f;
#pragma unroll
      for (int w = 0; w < 8; ++w) a += red[w * (4 * KVSZ) + o];
      kvp[(size_t)(rb * NH + cgl * 4) * KVSZ + o] = a;
    }
  }
}

__global__ __launch_bounds__(512, 2) void k1_kernel(const u16* __restrict__ x, const u16* __restrict__ wqkv,
                                                    const u16* __restrict__ wg, const u16* __restrict__ wu,
                                                    u16* __restrict__ pq, u16* __restrict__ h,
                                                    float* __restrict__ kvp) {
  extern __shared__ __attribute__((aligned(16))) char smem[];
  const int bid = blockIdx.x;
  const int lin = (bid & 7) * 30 + (bid >> 3);
  const int cg = lin >> 3, rb = lin & 7;
  if (cg < 18) k1_body<2>(x, wg, wu, h, kvp, rb, cg, smem);
  else if (cg < 26) k1_body<1>(x, wqkv, wqkv, pq, kvp, rb, cg - 18, smem);
  else k1_body<0>(x, wqkv, wqkv, pq, kvp, rb, cg - 26, smem);
}

__global__ __launch_bounds__(256) void k1b_kernel(const float* __restrict__ kvp, const u16* __restrict__ wo,
                                                  u16* __restrict__ wp, float* __restrict__ zout) {
  __shared__ float kv[KVSZ];
  const int h = blockIdx.x >> 3, j = blockIdx.x & 7;
  const int tid = threadIdx.x;
  for (int o = tid; o < KVSZ; o += 256) {
    float a = 0.f;
#pragma unroll
    for (int r = 0; r < 8; ++r) a += kvp[(size_t)(r * NH + h) * KVSZ + o];
    kv[o] = rbf(a);
  }
  __syncthreads();
  if (j == 0 && tid < DH) zout[h * DH + tid] = kv[DH * DH + tid];
  for (int o = tid; o < 20 * 40; o += 256) {
    int d = o / 40, n = (o % 40) * 2;
    float a0 = 0.f, a1 = 0.f;
#pragma unroll
    for (int e = 0; e < DH; ++e) {
      unsigned w = *(const unsigned*)(wo + (size_t)(h * DH + e) * D_ + j * 80 + n);
      float k = kv[d * DH + e];
      a0 += k * bflo(w);
      a1 += k * bfhi(w);
    }
    *(unsigned*)(wp + (size_t)(h * DH + d) * D_ + j * 80 + n) = pack2(a0, a1);
  }
}

constexpr int K2_NS = 8;
constexpr int K2_A = 64 * 64 * 2;
constexpr int K2_B = 64 * 80 * 2;
constexpr int K2_STAGE = K2_A + K2_B;
constexpr int K2_OFF_Z = K2_NS * K2_STAGE;
constexpr int K2_OFF_RD = K2_OFF_Z + 2560;
constexpr int K2_LDS = K2_OFF_RD + 8192;

__global__ __launch_bounds__(256, 1) void k2_kernel(const u16* __restrict__ x, const u16* __restrict__ h,
                                                    const u16* __restrict__ wd, const u16* __restrict__ pq,
                                                    const u16* __restrict__ wp, const float* __restrict__ zg,
                                                    u16* __restrict__ out) {
  extern __shared__ __attribute__((aligned(16))) char smem[];
  const int bid = blockIdx.x;
  const int lin = (bid & 7) * 32 + (bid >> 3);
  const int rb = lin >> 3, cb = lin & 7;
  const int m0 = rb * 64, n0 = cb * 80;
  const int tid = threadIdx.x;
  const int wid = __builtin_amdgcn_readfirstlane(tid >> 6);
  const int lane = tid & 63;
  const int g = lane >> 4, l15 = lane & 15;

  const u16* asrc = h + (size_t)(m0 + wid * 8 + (lane >> 3)) * F_ + (((lane & 7) ^ (lane >> 3)) * 8);
  int boffs[3];
#pragma unroll
  for (int i = 0; i < 3; ++i) {
    int p = wid + 4 * i;
    if (p >= 10) p = wid;
    int c = p * 64 + lane;
    int sub = c >> 4, kb = sub / 5, nb = sub % 5;
    int prow = (c >> 1) & 7, half = c & 1;
    int row = prow ^ ((kb & 1) << 2);
    boffs[i] = (8 * kb + row) * D_ + n0 + 16 * nb + 8 * half;
  }
  auto stageY = [&](int t, int buf) {
    char* sa = smem + buf * K2_STAGE;
    char* sb = sa + K2_A;
#pragma unroll
    for (int i = 0; i < 2; ++i) glds16(asrc + (size_t)i * 32 * F_ + t * 64, sa + (wid + 4 * i) * 1024);
#pragma unroll
    for (int i = 0; i < 3; ++i)
      if (wid + 4 * i < 10) glds16(wd + (size_t)t * 64 * D_ + boffs[i], sb + (wid + 4 * i) * 1024);
  };
  auto stageS = [&](int t, int buf) {
    char* sb = smem + buf * K2_STAGE + K2_A;
#pragma unroll
    for (int i = 0; i < 3; ++i)
      if (wid + 4 * i < 10) glds16(wp + (size_t)t * 64 * D_ + boffs[i], sb + (wid + 4 * i) * 1024);
  };

  const int arow = wid * 16 + l15;
  const int aoff0 = arow * 128 + ((g ^ (lane & 7)) * 16);
  const int bq = l15 >> 2, bp = lane & 3;
  const int boffc = g * 5 * 256 + bq * 32 + bp * 8;
  const int boff0 = boffc + 128 * (g & 1), boff1 = boffc + 128 * ((g & 1) ^ 1);

  f32x4 accy[5], accs[5];
#pragma unroll
  for (int j = 0; j < 5; ++j) { accy[j] = (f32x4){0.f, 0.f, 0.f, 0.f}; accs[j] = (f32x4){0.f, 0.f, 0.f, 0.f}; }

  constexpr int NTY = F_ / 64;
  constexpr int PF = K2_NS - 1;
#pragma unroll
  for (int t = 0; t < PF; ++t) stageY(t, t);
  for (int t = 0; t < NTY; ++t) {
    int rem = NTY - 1 - t;
    if (rem >= PF - 1) {
      if (wid < 2) { asm volatile("s_waitcnt vmcnt(%0)" ::"i"((PF - 1) * 5) : "memory"); }
      else { asm volatile("s_waitcnt vmcnt(%0)" ::"i"((PF - 1) * 4) : "memory"); }
    } else {
      WAIT_V(0);
    }
    BAR();
    if (t + PF < NTY) stageY(t + PF, (t + PF) % K2_NS);
    const char* sa = smem + (t % K2_NS) * K2_STAGE;
    const char* sb = sa + K2_A;
#pragma unroll
    for (int s = 0; s < 2; ++s) {
      bf16x8 xf = *(const bf16x8*)(sa + (aoff0 ^ (s * 64)));
#pragma unroll
      for (int j = 0; j < 5; ++j) {
        bf16x8 wf = tr_frag(sb + boff0 + (4 * s * 5 + j) * 256, sb + boff1 + (4 * s * 5 + j) * 256);
        accy[j] = __builtin_amdgcn_mfma_f32_16x16x32_bf16(wf, xf, accy[j], 0, 0, 0);
      }
    }
  }
  __syncthreads();
#pragma unroll
  for (int t = 0; t < PF; ++t) stageS(t, t);
  float* zs = (float*)(smem + K2_OFF_Z);
  float* rden = (float*)(smem + K2_OFF_RD);
  for (int o = tid; o < D_; o += 256) zs[o] = zg[o];
  __syncthreads();
  for (int o = tid; o < 64 * 32; o += 256) {
    int r = o >> 5, hh = o & 31;
    const u32x2* src = (const u32x2*)(pq + (size_t)(m0 + r) * D_ + hh * DH);
    float a = 0.f;
#pragma unroll
    for (int c = 0; c < 5; ++c) {
      u32x2 u = src[c];
      const float* z = zs + hh * DH + c * 4;
      a += bflo(u[0]) * z[0] + bfhi(u[0]) * z[1] + bflo(u[1]) * z[2] + bfhi(u[1]) * z[3];
    }
    rden[r * 32 + hh] = 1.0f / rbf(rbf(a) + 1e-6f);
  }
  __syncthreads();
  bf16x8 af[20];
  {
    const u16* prow = pq + (size_t)(m0 + arow) * D_ + 8 * g;
    const float* rd = rden + arow * 32;
#pragma unroll
    for (int s = 0; s < 20; ++s) {
      u32x4 u = *(const u32x4*)(prow + 32 * s);
      int k0 = 32 * s + 8 * g;
      int h0 = k0 / DH;
      int lim = (h0 + 1) * DH - k0;
      float r0 = rd[h0], r1 = rd[min(h0 + 1, 31)];
      float v[8];
#pragma unroll
      for (int e = 0; e < 4; ++e) {
        v[2 * e] = bflo(u[e]) * ((2 * e < lim) ? r0 : r1);
        v[2 * e + 1] = bfhi(u[e]) * ((2 * e + 1 < lim) ? r0 : r1);
      }
      u32x4 o = {pack2(v[0], v[1]), pack2(v[2], v[3]), pack2(v[4], v[5]), pack2(v[6], v[7])};
      af[s] = __builtin_bit_cast(bf16x8, o);
    }
  }
#pragma unroll
  for (int t = 0; t < 10; ++t) {
    WAIT_V(0);
    BAR();
    if (t + PF < 10) stageS(t + PF, (t + PF) % K2_NS);
    const char* sb = smem + (t % K2_NS) * K2_STAGE + K2_A;
#pragma unroll
    for (int s = 0; s < 2; ++s) {
#pragma unroll
      for (int j = 0; j < 5; ++j) {
        bf16x8 wf = tr_frag(sb + boff0 + (4 * s * 5 + j) * 256, sb + boff1 + (4 * s * 5 + j) * 256);
        accs[j] = __builtin_amdgcn_mfma_f32_16x16x32_bf16(wf, af[2 * t + s], accs[j], 0, 0, 0);
      }
    }
  }
#pragma unroll
  for (int j = 0; j < 5; ++j) {
    size_t o = (size_t)(m0 + arow) * D_ + n0 + j * 16 + 4 * g;
    u32x2 xv = *(const u32x2*)(x + o);
    float xs[4] = {bflo(xv[0]), bfhi(xv[0]), bflo(xv[1]), bfhi(xv[1])};
    float r[4];
#pragma unroll
    for (int e = 0; e < 4; ++e) {
      float y = rbf(accy[j][e]);
      float gt = rbf(y / (1.0f + __expf(-y)));
      float sv = rbf(accs[j][e]);
      r[e] = xs[e] + rbf(sv * gt);
    }
    u32x2 ov = {pack2(r[0], r[1]), pack2(r[2], r[3])};
    *(u32x2*)(out + o) = ov;
  }
}

extern "C" void kernel_launch(void* const* d_in, const int* in_sizes, int n_in, void* d_out, int out_size,
                              void* d_ws, size_t ws_size, hipStream_t stream) {
  const u16* x = (const u16*)d_in[0];
  const u16* wqkv = (const u16*)d_in[1];
  const u16* wo = (const u16*)d_in[2];
  const u16* wg = (const u16*)d_in[4];
  const u16* wu = (const u16*)d_in[5];
  const u16* wd = (const u16*)d_in[6];
  char* ws = (char*)d_ws;
  u16* pq = (u16*)(ws);
  u16* h = (u16*)(ws + (4 << 20));
  float* kvp = (float*)(ws + (12 << 20));
  u16* wp = (u16*)(ws + (13 << 20));
  float* zg = (float*)(ws + (14 << 20));
  hipFuncSetAttribute((const void*)k1_kernel, hipFuncAttributeMaxDynamicSharedMemorySize, K1_LDS);
  hipFuncSetAttribute((const void*)k2_kernel, hipFuncAttributeMaxDynamicSharedMemorySize, K2_LDS);
  k1_kernel<<<240, 512, K1_LDS, stream>>>(x, wqkv, wg, wu, pq, h, kvp);
  k1b_kernel<<<256, 256, 0, stream>>>(kvp, wo, wp, zg);
  k2_kernel<<<256, 256, K2_LDS, stream>>>(x, h, wd, pq, wp, zg, (u16*)d_out);
}
```

```cpp
#include <hip/hip_runtime.h>
#include <hip/hip_bf16.h>
#include <stdint.h>
#include <utility>

typedef unsigned short u16;
typedef __attribute__((ext_vector_type(8))) short bf16x8;
typedef __attribute__((ext_vector_type(4))) short s16x4;
typedef __attribute__((ext_vector_type(4))) float f32x4;
typedef __attribute__((ext_vector_type(2))) __bf16 bf2;
typedef __attribute__((ext_vector_type(4))) unsigned u32x4;
typedef __attribute__((ext_vector_type(2))) unsigned u32x2;
#define LAS __attribute__((address_space(3)))

constexpr int S_ = 2048, D_ = 640, F_ = 1728, NH = 32, DH = 20;
constexpr int KVSZ = DH * DH + DH;

__device__ __forceinline__ unsigned pack2(float a, float b) {
  bf2 v = {(__bf16)a, (__bf16)b};
  return __builtin_bit_cast(unsigned, v);
}
__device__ __forceinline__ float rbf(float a) { return (float)(__bf16)a; }
__device__ __forceinline__ float bflo(unsigned u) { return __uint_as_float(u << 16); }
__device__ __forceinline__ float bfhi(unsigned u) { return __uint_as_float(u & 0xffff0000u); }
__device__ __forceinline__ float dot_ones(unsigned a, float c) {
  bf2 one = {(__bf16)1.0f, (__bf16)1.0f};
  return __builtin_amdgcn_fdot2_f32_bf16(__builtin_bit_cast(bf2, a), one, c, false);
}
__device__ __forceinline__ float dot_self(unsigned a, float c) {
  bf2 v = __builtin_bit_cast(bf2, a);
  return __builtin_amdgcn_fdot2_f32_bf16(v, v, c, false);
}
__device__ __forceinline__ void glds16(const void* g, void* l) {
  __builtin_amdgcn_global_load_lds((const unsigned*)g, (unsigned*)l, 16, 0, 0);
}
template <class F, int... I> __device__ __forceinline__ void sfor_impl(F&& f, std::integer_sequence<int, I...>) {
  (f(std::integral_constant<int, I>{}), ...);
}
template <int N, class F> __device__ __forceinline__ void sfor(F&& f) { sfor_impl(f, std::make_integer_sequence<int, N>{}); }
#define WAIT_V(n) asm volatile("s_waitcnt vmcnt(" #n ")" ::: "memory")
#define BAR() __builtin_amdgcn_s_barrier()
template <int OFF> __device__ __forceinline__ s16x4 tr_rd(unsigned a) {
  s16x4 r; asm volatile("ds_read_b64_tr_b16 %0, %1 offset:%2" : "=&v"(r) : "v"(a), "i"(OFF) : "memory"); return r;
}
template <int OFF> __device__ __forceinline__ bf16x8 lds_rd128(unsigned a) {
  bf16x8 r; asm volatile("ds_read_b128 %0, %1 offset:%2" : "=&v"(r) : "v"(a), "i"(OFF) : "memory"); return r;
}
__device__ __forceinline__ bf16x8 cat8(s16x4 a, s16x4 b) { return __builtin_shufflevector(a, b, 0, 1, 2, 3, 4, 5, 6, 7); }
#define WAIT_L(n) do { asm volatile("s_waitcnt lgkmcnt(" #n ")" ::: "memory"); __builtin_amdgcn_sched_barrier(0); } while (0)
__device__ __forceinline__ unsigned lds_addr(const void* p) { return (unsigned)(size_t)(LAS const char*)p; }
__device__ __forceinline__ float phi_bf(float t) {
  float p = t > 0.f ? rbf(t + 1.0f) : rbf(__expf(t));
  return rbf(p + 1e-6f);
}

constexpr int K1_ABYTES = 256 * 64 * 2;
constexpr int K1_BBYTES = 64 * 192 * 2;
constexpr int K1_OFF_B = 3 * K1_ABYTES;
constexpr int K1_OFF_MISC = K1_OFF_B + 2 * K1_BBYTES;
constexpr int K1_OFF_DUMMY = K1_OFF_MISC;
constexpr int K1_OFF_MEAN = K1_OFF_DUMMY + 256;
constexpr int K1_OFF_CSF = K1_OFF_MEAN + 2048;
constexpr int K1_LDS = K1_OFF_CSF + 768;
constexpr int K1_OFF_RS = K1_ABYTES;
constexpr int K1_OFF_CS = 2 * K1_ABYTES;

template <int ROLE>
__device__ __forceinline__ void k1_body(const u16* __restrict__ x, const u16* __restrict__ w0,
                                        const u16* __restrict__ w1, u16* __restrict__ outp,
                                        float* __restrict__ kvp, int rb, int cgl, int q4, char* smem) {
  constexpr int BN = ROLE == 2 ? 192 : 160;
  constexpr int NB = BN / 16;
  constexpr int WM = ROLE == 1 ? 8 : 4;
  constexpr int WN = 8 / WM;
  constexpr int MT = 16 / WM;
  constexpr int NT = NB / WN;
  constexpr int BPIECES = 64 * BN * 2 / 1024;
  constexpr int LDW = ROLE == 2 ? F_ : 3 * D_;
  constexpr int HALFN = BN / 2;

  const int tid = threadIdx.x;
  const int wid = __builtin_amdgcn_readfirstlane(tid >> 6);
  const int lane = tid & 63;
  const int g = lane >> 4, l15 = lane & 15;
  const int wm = wid % WM, wn = wid / WM;
  const int m0 = rb * 256;
  const int CSOFF = 2 * wn;

  const char* abase = (const char*)(x + (size_t)m0 * D_);
  const unsigned aoffg = (unsigned)(((wid * 8 + (lane >> 3)) * D_ + (((lane & 7) ^ (lane >> 3)) * 8)) * 2);
  const char* bptr[3];
#pragma unroll
  for (int i = 0; i < 3; ++i) {
    int p = wid + 8 * i;
    if (p >= BPIECES) p = wid;
    int c = p * 64 + lane;
    int sub = c >> 4, kb = sub / NB, nb = sub % NB;
    int prow = (c >> 1) & 7, half = c & 1;
    int row = prow ^ ((kb & 1) << 2);
    int k = 8 * kb + row, n = 16 * nb + 8 * half;
    const u16* wp;
    if (ROLE == 0) wp = w0 + cgl * 160 + n;
    else if (ROLE == 1) wp = (n < HALFN) ? (w0 + D_ + cgl * 80 + n) : (w0 + 2 * D_ + cgl * 80 + n - HALFN);
    else wp = (n < HALFN) ? (w0 + cgl * 96 + n) : (w1 + cgl * 96 + n - HALFN);
    bptr[i] = (const char*)(wp + (size_t)k * LDW);
  }
  auto loadA = [&](int t, int slot, int i) {
    glds16(abase + (size_t)(i * 64 * D_ * 2 + t * 128) + aoffg, smem + slot * K1_ABYTES + (wid + 8 * i) * 1024);
  };
  auto loadB = [&](int t, int slot, int i) {
    if (wid + 8 * i < BPIECES) {
      glds16(bptr[i] + (size_t)t * (64 * LDW * 2), smem + K1_OFF_B + slot * K1_BBYTES + (wid + 8 * i) * 1024);
    }
  };
  auto stageA = [&](int t, int slot) {
#pragma unroll
    for (int i = 0; i < 4; ++i) loadA(t, slot, i);
  };
  auto stageB = [&](int t, int slot) {
#pragma unroll
    for (int i = 0; i < 3; ++i) loadB(t, slot, i);
  };

  const char* pfp;
  if (wid < 4) {
    pfp = abase + (size_t)((64 * q4 + 16 * wid + (lane >> 2)) * D_ * 2 + (lane & 1) * 64);
  } else {
    const int e = lane & 7;
    const u16* wp;
    if (ROLE == 0) wp = w0 + cgl * 160 + min(e, 4) * 32;
    else if (ROLE == 1) wp = (e < 4) ? (w0 + D_ + cgl * 80 + min(e * 32, 78)) : (w0 + 2 * D_ + cgl * 80 + min((e - 4) * 32, 78));
    else wp = (e < 4) ? (w0 + cgl * 96 + min(e, 2) * 32) : (w1 + cgl * 96 + min(e - 4, 2) * 32);
    pfp = (const char*)(wp + (size_t)(8 * rb + 2 * (wid - 4) + (lane >> 5)) * LDW);
  }
  const unsigned pfstep = wid < 4 ? 128u : (unsigned)(64 * LDW * 2);
  auto prefetch = [&](int t) {
    if (t > 9) t = 9;
    __builtin_amdgcn_global_load_lds((const unsigned*)(pfp + (size_t)t * pfstep), (unsigned*)(smem + K1_OFF_DUMMY), 4, 0, 0);
  };
  stageA(0, 0);
  stageB(0, 0);
  stageA(1, 1);
  prefetch(2);
  prefetch(3);

  const int arow = wm * (MT * 16) + l15;
  const unsigned lbase = lds_addr(smem);
  const unsigned aoff0 = lbase + arow * 128 + ((g ^ (lane & 7)) * 16);
  const int bq = l15 >> 2, bp = lane & 3;
  const unsigned boffc = lbase + K1_OFF_B + g * NB * 256 + bq * 32 + bp * 8 + wn * (ROLE == 2 ? 3 : NT) * 256;
  const unsigned boff0 = boffc + 128 * (g & 1), boff1 = boffc + 128 * ((g & 1) ^ 1);

  f32x4 acc[MT][NT];
#pragma unroll
  for (int i = 0; i < MT; ++i)
#pragma unroll
    for (int j = 0; j < NT; ++j) acc[i][j] = (f32x4){0.f, 0.f, 0.f, 0.f};
  float rs[MT], rq[MT], cs[NT];
#pragma unroll
  for (int i = 0; i < MT; ++i) { rs[i] = 0.f; rq[i] = 0.f; }
#pragma unroll
  for (int j = 0; j < NT; ++j) cs[j] = 0.f;

  bf16x8 xf[2][MT];
  s16x4 wl[2][NT], wh[2][NT];
  auto rd = [&](auto S, unsigned soa, unsigned sob) {
    constexpr int s = S.value;
    const unsigned aa = (aoff0 ^ (s * 64)) + soa;
    sfor<MT>([&](auto I) { xf[s][I.value] = lds_rd128<I.value * 2048>((I.value & 1) ? aa - wn * 2048 : aa + wn * 2048); });
    sfor<NT>([&](auto J) {
      constexpr int j = J.value;
      constexpr int nb = (ROLE == 2) ? (j < 3 ? j : 3 + j) : j;
      wl[s][j] = tr_rd<(4 * s * NB + nb) * 256>(boff0 + sob);
      wh[s][j] = tr_rd<(4 * s * NB + nb) * 256>(boff1 + sob);
    });
  };
  auto mm = [&](auto S, auto LDX, int t, int nslot, bool do_cs) {
    constexpr int s = S.value, LD = LDX.value;
    bf16x8 wf[NT];
#pragma unroll
    for (int j = 0; j < NT; ++j) wf[j] = cat8(wl[s][j], wh[s][j]);
    constexpr int NM = MT * NT;
    constexpr int ND = (WN == 2) ? (MT / 2) * 8 : MT * 8;
    sfor<NM>([&](auto X) {
      constexpr int xi = X.value, i = xi / NT, j = xi % NT;
      if (ROLE == 1) acc[i][j] = __builtin_amdgcn_mfma_f32_16x16x32_bf16(xf[s][i], wf[j], acc[i][j], 0, 0, 0);
      else acc[i][j] = __builtin_amdgcn_mfma_f32_16x16x32_bf16(wf[j], xf[s][i], acc[i][j], 0, 0, 0);
      constexpr int d0 = xi * ND / NM, d1 = (xi + 1) * ND / NM;
      sfor<d1 - d0>([&](auto Y) {
        constexpr int d = d0 + Y.value;
        constexpr int NTL = ND / 8;
        constexpr int q = (d >> 1) % NTL, e = (d >> 1) / NTL;
        constexpr int ii = (WN == 2) ? 2 * q : q;
        unsigned u = __builtin_bit_cast(u32x4, xf[s][ii])[e];
        if (d & 1) rq[q] = dot_self(u, rq[q]); else rs[q] = dot_ones(u, rs[q]);
      });
      constexpr int P = NM / 4;
      if constexpr (LD == 0 && (xi == 1 || xi == 1 + P || xi == 1 + 2 * P)) { if (t + 1 < 10) loadB(t + 1, (t + 1) & 1, (xi - 1) / P); }
      if constexpr (LD == 0 && xi == 1 + 3 * P) { if (t + 2 < 10) loadA(t + 2, nslot, 0); }
      if constexpr (LD == 1 && (xi == 1 || xi == 1 + P || xi == 1 + 2 * P)) { if (t + 2 < 10) loadA(t + 2, nslot, 1 + (xi - 1) / P); }
      if constexpr (LD == 1 && xi == 1 + 3 * P) prefetch(t + 4);
      __builtin_amdgcn_sched_barrier(0);
    });
    if (do_cs) {
#pragma unroll
      for (int j = 0; j < NT; ++j) {
        u32x4 u = __builtin_bit_cast(u32x4, wf[j]);
#pragma unroll
        for (int e = 0; e < 4; ++e) cs[j] = dot_ones(u[e], cs[j]);
      }
    }
  };
  using I0 = std::integral_constant<int, 0>; using I1 = std::integral_constant<int, 1>; using I2 = std::integral_constant<int, 2>;
  constexpr int NRD = MT + 2 * NT;

  int aslot = 0;
  if (wid < 4) {
#pragma nounroll
    for (int t = 0; t < 10; ++t) {
      if (t == 9) WAIT_V(0); else if (t == 0) WAIT_V(6); else WAIT_V(5);
      BAR();
      const int nslot = aslot == 0 ? 2 : aslot - 1;
      const unsigned soa = aslot * K1_ABYTES, sob = (t & 1) * K1_BBYTES;
      aslot = aslot == 2 ? 0 : aslot + 1;
      const bool do_cs = ((t + CSOFF) % WM) == wm;
      rd(I0{}, soa, sob);
      rd(I1{}, soa, sob);
      if (NRD > 15) WAIT_L(15); else WAIT_L(14);
      mm(I0{}, I0{}, t, nslot, do_cs);
      WAIT_L(0);
      mm(I1{}, I1{}, t, nslot, do_cs);
    }
  } else {
    bool prev_cs = (CSOFF % WM) == wm;
    {
      WAIT_V(6);
      BAR();
      rd(I0{}, 0u, 0u);
      stageB(1, 1);
      loadA(2, 2, 0);
      WAIT_L(0);
      rd(I1{}, 0u, 0u);
      mm(I0{}, I1{}, 0, 2, prev_cs);
      WAIT_L(0);
      aslot = 1;
    }
#pragma nounroll
    for (int t = 1; t < 10; ++t) {
      if (t == 9) WAIT_V(0); else WAIT_V(5);
      BAR();
      const int nslot = aslot == 0 ? 2 : aslot - 1;
      const unsigned soa = aslot * K1_ABYTES, sob = (t & 1) * K1_BBYTES;
      aslot = aslot == 2 ? 0 : aslot + 1;
      const bool do_cs = ((t + CSOFF) % WM) == wm;
      rd(I0{}, soa, sob);
      mm(I1{}, I0{}, t, nslot, prev_cs);
      WAIT_L(0);
      rd(I1{}, soa, sob);
      mm(I0{}, I1{}, t, nslot, do_cs);
      WAIT_L(0);
      prev_cs = do_cs;
    }
    mm(I1{}, I2{}, 10, 0, prev_cs);
  }

  float* rsum = (float*)(smem + K1_OFF_RS);
  float* csum = (float*)(smem + K1_OFF_CS);
  float* smean = (float*)(smem + K1_OFF_MEAN);
  float* srsd = smean + 256;
  float* scs = (float*)(smem + K1_OFF_CSF);
#pragma unroll
  for (int q = 0; q < (WN == 2 ? MT / 2 : MT); ++q) {
    int r = wm * (MT * 16) + ((WN == 2) ? ((2 * q) ^ wn) : q) * 16 + l15;
    float2 v = {rs[q], rq[q]};
    *(float2*)(rsum + (g * 256 + r) * 2) = v;
  }
#pragma unroll
  for (int j = 0; j < NT; ++j) {
    int nb = (ROLE == 2) ? ((j < 3) ? (wn * 3 + j) : (6 + wn * 3 + (j - 3))) : (wn * NT + j);
    csum[(wm * 4 + g) * 192 + nb * 16 + l15] = cs[j];
  }
  __syncthreads();
  if (tid < 256) {
    float a = 0.f, b = 0.f;
#pragma unroll
    for (int w = 0; w < 4; ++w) { float2 v = *(const float2*)(rsum + (w * 256 + tid) * 2); a += v.x; b += v.y; }
    float mean = a * (1.0f / D_);
    float var = fmaxf(b * (1.0f / D_) - mean * mean, 0.f);
    float sd = rbf(sqrtf(rbf(rbf(var) + 1e-5f)));
    smean[tid] = rbf(mean);
    srsd[tid] = 1.0f / sd;
  } else if (tid - 256 < BN) {
    int n = tid - 256;
    float a = 0.f;
#pragma unroll
    for (int w = 0; w < WM * 4; ++w) a += csum[w * 192 + n];
    scs[n] = a;
  }
  __syncthreads();

  if (ROLE == 0) {
    constexpr int PITCH = 336;
#pragma unroll
    for (int i = 0; i < MT; ++i) {
      int r = wm * 64 + (i ^ wn) * 16 + l15;
      float mean = smean[r], rsd = srsd[r];
#pragma unroll
      for (int j = 0; j < NT; ++j) {
        int n = (wn * NT + j) * 16 + 4 * g;
        f32x4 c4 = *(const f32x4*)(scs + n);
        float p[4];
#pragma unroll
        for (int e = 0; e < 4; ++e) p[e] = phi_bf(rbf((acc[i][j][e] - mean * c4[e]) * rsd));
        u32x2 o = {pack2(p[0], p[1]), pack2(p[2], p[3])};
        *(u32x2*)(smem + r * PITCH + n * 2) = o;
      }
    }
    __syncthreads();
#pragma unroll
    for (int it = 0; it < 10; ++it) {
      int o = tid + 512 * it, r = o / 20, c = o - r * 20;
      u32x4 v = *(const u32x4*)(smem + r * PITCH + c * 16);
      *(u32x4*)(outp + (size_t)(m0 + r) * D_ + cgl * 160 + c * 8) = v;
    }
  } else if (ROLE == 2) {
    constexpr int PITCH = 208;
#pragma unroll
    for (int i = 0; i < MT; ++i) {
      int r = wm * 64 + (i ^ wn) * 16 + l15;
      float mean = smean[r], rsd = srsd[r];
#pragma unroll
      for (int j = 0; j < 3; ++j) {
        int n = (wn * 3 + j) * 16 + 4 * g;
        f32x4 cg4 = *(const f32x4*)(scs + n);
        f32x4 cu4 = *(const f32x4*)(scs + 96 + n);
        float hv[4];
#pragma unroll
        for (int e = 0; e < 4; ++e) {
          float gv = rbf((acc[i][j][e] - mean * cg4[e]) * rsd);
          float uv = rbf((acc[i][j + 3][e] - mean * cu4[e]) * rsd);
          float sg = rbf(__builtin_amdgcn_rcpf(1.0f + __expf(-gv)));
          hv[e] = sg * uv;
        }
        u32x2 o = {pack2(hv[0], hv[1]), pack2(hv[2], hv[3])};
        *(u32x2*)(smem + r * PITCH + n * 2) = o;
      }
    }
    __syncthreads();
#pragma unroll
    for (int it = 0; it < 6; ++it) {
      int o = tid + 512 * it, r = o / 12, c = o - r * 12;
      u32x4 v = *(const u32x4*)(smem + r * PITCH + c * 16);
      *(u32x4*)(outp + (size_t)(m0 + r) * F_ + cgl * 96 + c * 8) = v;
    }
  } else {
    bf16x8 fr[10];
    float mean[2][4], rsd[2][4];
#pragma unroll
    for (int i = 0; i < 2; ++i)
#pragma unroll
      for (int e = 0; e < 4; ++e) {
        int r = wid * 32 + i * 16 + 4 * g + e;
        mean[i][e] = smean[r];
        rsd[i][e] = srsd[r];
      }
#pragma unroll
    for (int j = 0; j < 10; ++j) {
      float c = scs[j * 16 + l15];
      float v[8];
#pragma unroll
      for (int i = 0; i < 2; ++i)
#pragma unroll
        for (int e = 0; e < 4; ++e) {
          float t = rbf((acc[i][j][e] - mean[i][e] * c) * rsd[i][e]);
          v[i * 4 + e] = (j < 5) ? phi_bf(t) : t;
        }
      u32x4 u = {pack2(v[0], v[1]), pack2(v[2], v[3]), pack2(v[4], v[5]), pack2(v[6], v[7])};
      fr[j] = __builtin_bit_cast(bf16x8, u);
    }
    float* red = (float*)smem;
    float* myred = red + wid * (4 * KVSZ);
    const bf16x8 ones = {0x3F80, 0x3F80, 0x3F80, 0x3F80, 0x3F80, 0x3F80, 0x3F80, 0x3F80};
#pragma unroll
    for (int i = 0; i < 5; ++i) {
#pragma unroll
      for (int j = 0; j < 5; ++j) {
        if (j < i - 1 || j > i + 1) continue;
        f32x4 z4 = {0.f, 0.f, 0.f, 0.f};
        f32x4 d = __builtin_amdgcn_mfma_f32_16x16x32_bf16(fr[i], fr[5 + j], z4, 0, 0, 0);
        int vc = 16 * j + l15, hv = vc / DH, ev = vc - hv * DH;
#pragma unroll
        for (int e = 0; e < 4; ++e) {
          int kc = 16 * i + 4 * g + e, hk = kc / DH, dk = kc - hk * DH;
          if (hk == hv) myred[hk * KVSZ + dk * DH + ev] = d[e];
        }
      }
      f32x4 z4 = {0.f, 0.f, 0.f, 0.f};
      f32x4 d = __builtin_amdgcn_mfma_f32_16x16x32_bf16(fr[i], ones, z4, 0, 0, 0);
      if (l15 == 0) {
#pragma unroll
        for (int e = 0; e < 4; ++e) {
          int kc = 16 * i + 4 * g + e, hk = kc / DH, dk = kc - hk * DH;
          myred[hk * KVSZ + DH * DH + dk] = d[e];
        }
      }
    }
    __syncthreads();
    for (int o = tid; o < 4 * KVSZ; o += 512) {
      float a = 0.f;
#pragma unroll
      for (int w = 0; w < 8; ++w) a += red[w * (4 * KVSZ) + o];
      kvp[(size_t)(rb * NH + cgl * 4) * KVSZ + o] = a;
    }
  }
}

__global__ __launch_bounds__(512, 2) void k1_kernel(const u16* __restrict__ x, const u16* __restrict__ wqkv,
                                                    const u16* __restrict__ wg, const u16* __restrict__ wu,
                                                    u16* __restrict__ pq, u16* __restrict__ h,
                                                    float* __restrict__ kvp) {
  extern __shared__ __attribute__((aligned(16))) char smem[];
  const int bid = blockIdx.x;
  const int lin = (bid & 7) * 30 + (bid >> 3);
  const int cg = lin >> 3, rb = lin & 7;
  if (cg < 18) k1_body<2>(x, wg, wu, h, kvp, rb, cg, cg & 3, smem);
  else if (cg < 26) k1_body<1>(x, wqkv, wqkv, pq, kvp, rb, cg - 18, cg & 3, smem);
  else k1_body<0>(x, wqkv, wqkv, pq, kvp, rb, cg - 26, cg & 3, smem);
}

__global__ __launch_bounds__(256) void k1b_kernel(const float* __restrict__ kvp, const u16* __restrict__ wo,
                                                  u16* __restrict__ wp, float* __restrict__ zout) {
  __shared__ float kv[KVSZ];
  const int h = blockIdx.x >> 3, j = blockIdx.x & 7;
  const int tid = threadIdx.x;
  for (int o = tid; o < KVSZ; o += 256) {
    float a = 0.f;
#pragma unroll
    for (int r = 0; r < 8; ++r) a += kvp[(size_t)(r * NH + h) * KVSZ + o];
    kv[o] = rbf(a);
  }
  __syncthreads();
  if (j == 0 && tid < DH) zout[h * DH + tid] = kv[DH * DH + tid];
  for (int o = tid; o < 20 * 40; o += 256) {
    int d = o / 40, n = (o % 40) * 2;
    float a0 = 0.f, a1 = 0.f;
#pragma unroll
    for (int e = 0; e < DH; ++e) {
      unsigned w = *(const unsigned*)(wo + (size_t)(h * DH + e) * D_ + j * 80 + n);
      float k = kv[d * DH + e];
      a0 += k * bflo(w);
      a1 += k * bfhi(w);
    }
    *(unsigned*)(wp + (size_t)(h * DH + d) * D_ + j * 80 + n) = pack2(a0, a1);
  }
}

constexpr int K2_NS = 8;
constexpr int K2_A = 64 * 64 * 2;
constexpr int K2_B = 64 * 80 * 2;
constexpr int K2_STAGE = K2_A + K2_B;
constexpr int K2_OFF_Z = K2_NS * K2_STAGE;
constexpr int K2_OFF_RD = K2_OFF_Z + 2560;
constexpr int K2_LDS = K2_OFF_RD + 8192;

__global__ __launch_bounds__(256, 1) void k2_kernel(const u16* __restrict__ x, const u16* __restrict__ h,
                                                    const u16* __restrict__ wd, const u16* __restrict__ pq,
                                                    const u16* __restrict__ wp, const float* __restrict__ zg,
                                                    u16* __restrict__ out) {
  extern __shared__ __attribute__((aligned(16))) char smem[];
  const int bid = blockIdx.x;
  const int lin = (bid & 7) * 32 + (bid >> 3);
  const int rb = lin >> 3, cb = lin & 7;
  const int m0 = rb * 64, n0 = cb * 80;
  const int tid = threadIdx.x;
  const int wid = __builtin_amdgcn_readfirstlane(tid >> 6);
  const int lane = tid & 63;
  const int g = lane >> 4, l15 = lane & 15;

  const u16* asrc = h + (size_t)(m0 + wid * 8 + (lane >> 3)) * F_ + (((lane & 7) ^ (lane >> 3)) * 8);
  int boffs[3];
#pragma unroll
  for (int i = 0; i < 3; ++i) {
    int p = wid + 4 * i;
    if (p >= 10) p = wid;
    int c = p * 64 + lane;
    int sub = c >> 4, kb = sub / 5, nb = sub % 5;
    int prow = (c >> 1) & 7, half = c & 1;
    int row = prow ^ ((kb & 1) << 2);
    boffs[i] = (8 * kb + row) * D_ + n0 + 16 * nb + 8 * half;
  }
  auto stageY = [&](int t, int buf) {
    char* sa = smem + buf * K2_STAGE;
    char* sb = sa + K2_A;
#pragma unroll
    for (int i = 0; i < 2; ++i) glds16(asrc + (size_t)i * 32 * F_ + t * 64, sa + (wid + 4 * i) * 1024);
#pragma unroll
    for (int i = 0; i < 3; ++i)
      if (wid + 4 * i < 10) glds16(wd + (size_t)t * 64 * D_ + boffs[i], sb + (wid + 4 * i) * 1024);
  };
  auto stageS = [&](int t, int buf) {
    char* sb = smem + buf * K2_STAGE + K2_A;
#pragma unroll
    for (int i = 0; i < 3; ++i)
      if (wid + 4 * i < 10) glds16(wp + (size_t)t * 64 * D_ + boffs[i], sb + (wid + 4 * i) * 1024);
  };

  const int arow = wid * 16 + l15;
  const unsigned lbase = lds_addr(smem);
  const unsigned aoff0 = lbase + arow * 128 + ((g ^ (lane & 7)) * 16);
  const int bq = l15 >> 2, bp = lane & 3;
  const unsigned boffc = lbase + K2_A + g * 5 * 256 + bq * 32 + bp * 8;
  const unsigned boff0 = boffc + 128 * (g & 1), boff1 = boffc + 128 * ((g & 1) ^ 1);

  f32x4 accy[5], accs[5];
#pragma unroll
  for (int j = 0; j < 5; ++j) { accy[j] = (f32x4){0.f, 0.f, 0.f, 0.f}; accs[j] = (f32x4){0.f, 0.f, 0.f, 0.f}; }

  constexpr int NTY = F_ / 64;
  constexpr int PF = K2_NS - 1;
#pragma unroll
  for (int t = 0; t < PF; ++t) stageY(t, t);
  for (int t = 0; t < NTY; ++t) {
    int rem = NTY - 1 - t;
    if (rem >= PF - 1) {
      if (wid < 2) { asm volatile("s_waitcnt vmcnt(%0)" ::"i"((PF - 1) * 5) : "memory"); }
      else { asm volatile("s_waitcnt vmcnt(%0)" ::"i"((PF - 1) * 4) : "memory"); }
    } else {
      WAIT_V(0);
    }
    BAR();
    if (t + PF < NTY) stageY(t + PF, (t + PF) % K2_NS);
    const unsigned so = (t % K2_NS) * K2_STAGE;
    bf16x8 xf[2];
    s16x4 wl[2][5], wh[2][5];
    sfor<2>([&](auto S) {
      constexpr int s = S.value;
      xf[s] = lds_rd128<0>((aoff0 ^ (s * 64)) + so);
      sfor<5>([&](auto J) {
        constexpr int j = J.value;
        wl[s][j] = tr_rd<(4 * s * 5 + j) * 256>(boff0 + so);
        wh[s][j] = tr_rd<(4 * s * 5 + j) * 256>(boff1 + so);
      });
    });
#pragma unroll
    for (int s = 0; s < 2; ++s) {
      if (s == 0) WAIT_L(11); else WAIT_L(0);
#pragma unroll
      for (int j = 0; j < 5; ++j)
        accy[j] = __builtin_amdgcn_mfma_f32_16x16x32_bf16(cat8(wl[s][j], wh[s][j]), xf[s], accy[j], 0, 0, 0);
    }
  }
  __syncthreads();
#pragma unroll
  for (int t = 0; t < PF; ++t) stageS(t, t);
  float* zs = (float*)(smem + K2_OFF_Z);
  float* rden = (float*)(smem + K2_OFF_RD);
  for (int o = tid; o < D_; o += 256) zs[o] = zg[o];
  __syncthreads();
  for (int o = tid; o < 64 * 32; o += 256) {
    int r = o >> 5, hh = o & 31;
    const u32x2* src = (const u32x2*)(pq + (size_t)(m0 + r) * D_ + hh * DH);
    float a = 0.f;
#pragma unroll
    for (int c = 0; c < 5; ++c) {
      u32x2 u = src[c];
      const float* z = zs + hh * DH + c * 4;
      a += bflo(u[0]) * z[0] + bfhi(u[0]) * z[1] + bflo(u[1]) * z[2] + bfhi(u[1]) * z[3];
    }
    rden[r * 32 + hh] = 1.0f / rbf(rbf(a) + 1e-6f);
  }
  __syncthreads();
  bf16x8 af[20];
  {
    const u16* prow = pq + (size_t)(m0 + arow) * D_ + 8 * g;
    const float* rd = rden + arow * 32;
#pragma unroll
    for (int s = 0; s < 20; ++s) {
      u32x4 u = *(const u32x4*)(prow + 32 * s);
      int k0 = 32 * s + 8 * g;
      int h0 = k0 / DH;
      int lim = (h0 + 1) * DH - k0;
      float r0 = rd[h0], r1 = rd[min(h0 + 1, 31)];
      float v[8];
#pragma unroll
      for (int e = 0; e < 4; ++e) {
        v[2 * e] = bflo(u[e]) * ((2 * e < lim) ? r0 : r1);
        v[2 * e + 1] = bfhi(u[e]) * ((2 * e + 1 < lim) ? r0 : r1);
      }
      u32x4 o = {pack2(v[0], v[1]), pack2(v[2], v[3]), pack2(v[4], v[5]), pack2(v[6], v[7])};
      af[s] = __builtin_bit_cast(bf16x8, o);
    }
  }
#pragma unroll
  for (int t = 0; t < 10; ++t) {
    WAIT_V(0);
    BAR();
    if (t + PF < 10) stageS(t + PF, (t + PF) % K2_NS);
    const unsigned so = (t % K2_NS) * K2_STAGE;
    s16x4 wl[2][5], wh[2][5];
    sfor<2>([&](auto S) {
      constexpr int s = S.value;
      sfor<5>([&](auto J) {
        constexpr int j = J.value;
        wl[s][j] = tr_rd<(4 * s * 5 + j) * 256>(boff0 + so);
        wh[s][j] = tr_rd<(4 * s * 5 + j) * 256>(boff1 + so);
      });
    });
#pragma unroll
    for (int s = 0; s < 2; ++s) {
      if (s == 0) WAIT_L(10); else WAIT_L(0);
#pragma unroll
      for (int j = 0; j < 5; ++j)
        accs[j] = __builtin_amdgcn_mfma_f32_16x16x32_bf16(cat8(wl[s][j], wh[s][j]), af[2 * t + s], accs[j], 0, 0, 0);
    }
  }
#pragma unroll
  for (int j = 0; j < 5; ++j) {
    size_t o = (size_t)(m0 + arow) * D_ + n0 + j * 16 + 4 * g;
    u32x2 xv = *(const u32x2*)(x + o);
    float xs[4] = {bflo(xv[0]), bfhi(xv[0]), bflo(xv[1]), bfhi(xv[1])};
    float r[4];
#pragma unroll
    for (int e = 0; e < 4; ++e) {
      float y = rbf(accy[j][e]);
      float gt = rbf(y / (1.0f + __expf(-y)));
      float sv = rbf(accs[j][e]);
      r[e] = xs[e] + rbf(sv * gt);
    }
    u32x2 ov = {pack2(r[0], r[1]), pack2(r[2], r[3])};
    *(u32x2*)(out + o) = ov;
  }
}

extern "C" void kernel_launch(void* const* d_in, const int* in_sizes, int n_in, void* d_out, int out_size,
                              void* d_ws, size_t ws_size, hipStream_t stream) {
  const u16* x = (const u16*)d_in[0];
  const u16* wqkv = (const u16*)d_in[1];
  const u16* wo = (const u16*)d_in[2];
  const u16* wg = (const u16*)d_in[4];
  const u16* wu = (const u16*)d_in[5];
  const u16* wd = (const u16*)d_in[6];
  char* ws = (char*)d_ws;
  u16* pq = (u16*)(ws);
  u16* h = (u16*)(ws + (4 << 20));
  float* kvp = (float*)(ws + (12 << 20));
  u16* wp = (u16*)(ws + (13 << 20));
  float* zg = (float*)(ws + (14 << 20));
  hipFuncSetAttribute((const void*)k1_kernel, hipFuncAttributeMaxDynamicSharedMemorySize, K1_LDS);
  hipFuncSetAttribute((const void*)k2_kernel, hipFuncAttributeMaxDynamicSharedMemorySize, K2_LDS);
  k1_kernel<<<240, 512, K1_LDS, stream>>>(x, wqkv, wg, wu, pq, h, kvp);
  k1b_kernel<<<256, 256, 0, stream>>>(kvp, wo, wp, zg);
  k2_kernel<<<256, 256, K2_LDS, stream>>>(x, h, wd, pq, wp, zg, (u16*)d_out);
}
```

```cpp
#include <hip/hip_runtime.h>
#include <hip/hip_bf16.h>
#include <stdint.h>
#include <utility>

typedef unsigned short u16;
typedef __attribute__((ext_vector_type(8))) short bf16x8;
typedef __attribute__((ext_vector_type(4))) short s16x4;
typedef __attribute__((ext_vector_type(4))) float f32x4;
typedef __attribute__((ext_vector_type(2))) __bf16 bf2;
typedef __attribute__((ext_vector_type(4))) unsigned u32x4;
typedef __attribute__((ext_vector_type(2))) unsigned u32x2;
#define LAS __attribute__((address_space(3)))

constexpr int S_ = 2048, D_ = 640, F_ = 1728, NH = 32, DH = 20;
constexpr int KVSZ = DH * DH + DH;

__device__ __forceinline__ unsigned pack2(float a, float b) {
  bf2 v = {(__bf16)a, (__bf16)b};
  return __builtin_bit_cast(unsigned, v);
}
__device__ __forceinline__ float rbf(float a) { return (float)(__bf16)a; }
__device__ __forceinline__ float bflo(unsigned u) { return __uint_as_float(u << 16); }
__device__ __forceinline__ float bfhi(unsigned u) { return __uint_as_float(u & 0xffff0000u); }
__device__ __forceinline__ float dot_ones(unsigned a, float c) {
  bf2 one = {(__bf16)1.0f, (__bf16)1.0f};
  return __builtin_amdgcn_fdot2_f32_bf16(__builtin_bit_cast(bf2, a), one, c, false);
}
__device__ __forceinline__ float dot_self(unsigned a, float c) {
  bf2 v = __builtin_bit_cast(bf2, a);
  return __builtin_amdgcn_fdot2_f32_bf16(v, v, c, false);
}
__device__ __forceinline__ void glds16(const void* g, void* l) {
  __builtin_amdgcn_global_load_lds((const unsigned*)g, (unsigned*)l, 16, 0, 0);
}
template <class F, int... I> __device__ __forceinline__ void sfor_impl(F&& f, std::integer_sequence<int, I...>) {
  (f(std::integral_constant<int, I>{}), ...);
}
template <int N, class F> __device__ __forceinline__ void sfor(F&& f) { sfor_impl(f, std::make_integer_sequence<int, N>{}); }
#define WAIT_V(n) asm volatile("s_waitcnt vmcnt(" #n ")" ::: "memory")
#define BAR() __builtin_amdgcn_s_barrier()
template <int OFF> __device__ __forceinline__ s16x4 tr_rd(unsigned a) {
  s16x4 r; asm volatile("ds_read_b64_tr_b16 %0, %1 offset:%2" : "=&v"(r) : "v"(a), "i"(OFF) : "memory"); return r;
}
template <int OFF> __device__ __forceinline__ bf16x8 lds_rd128(unsigned a) {
  bf16x8 r; asm volatile("ds_read_b128 %0, %1 offset:%2" : "=&v"(r) : "v"(a), "i"(OFF) : "memory"); return r;
}
template <int OFF> __device__ __forceinline__ bf16x8 gld128(const void* sbase, unsigned voff) {
  bf16x8 r; asm volatile("global_load_dwordx4 %0, %1, %2 offset:%3" : "=&v"(r) : "v"(voff), "s"(sbase), "i"(OFF) : "memory"); return r;
}
template <int OFF> __device__ __forceinline__ bf16x8 gld128a(const void* sbase, unsigned voff) {
  bf16x8 r; asm volatile("global_load_dwordx4 %0, %1, %2 offset:%3" : "=&a"(r) : "v"(voff), "s"(sbase), "i"(OFF) : "memory"); return r;
}
__device__ __forceinline__ bf16x8 cat8(s16x4 a, s16x4 b) { return __builtin_shufflevector(a, b, 0, 1, 2, 3, 4, 5, 6, 7); }
#define WAIT_L(n) do { asm volatile("s_waitcnt lgkmcnt(" #n ")" ::: "memory"); __builtin_amdgcn_sched_barrier(0); } while (0)
__device__ __forceinline__ unsigned lds_addr(const void* p) { return (unsigned)(size_t)(LAS const char*)p; }
__device__ __forceinline__ float phi_bf(float t) {
  float p = t > 0.f ? rbf(t + 1.0f) : rbf(__expf(t));
  return rbf(p + 1e-6f);
}

constexpr int K1_ABYTES = 256 * 64 * 2;
constexpr int K1_BBYTES = 64 * 192 * 2;
constexpr int K1_OFF_B = 3 * K1_ABYTES;
constexpr int K1_OFF_MISC = K1_OFF_B + 2 * K1_BBYTES;
constexpr int K1_OFF_DUMMY = K1_OFF_MISC;
constexpr int K1_OFF_MEAN = K1_OFF_DUMMY + 256;
constexpr int K1_OFF_CSF = K1_OFF_MEAN + 2048;
constexpr int K1_LDS = K1_OFF_CSF + 768;
constexpr int K1_OFF_RS = K1_ABYTES;
constexpr int K1_OFF_CS = 2 * K1_ABYTES;

template <int ROLE>
__device__ __forceinline__ void k1_body(const u16* __restrict__ x, const u16* __restrict__ w0,
                                        const u16* __restrict__ w1, u16* __restrict__ outp,
                                        float* __restrict__ kvp, int rb, int cgl, int q4, char* smem) {
  constexpr int BN = ROLE == 2 ? 192 : 160;
  constexpr int NB = BN / 16;
  constexpr int WM = ROLE == 1 ? 8 : 4;
  constexpr int WN = 8 / WM;
  constexpr int MT = 16 / WM;
  constexpr int NT = NB / WN;
  constexpr int BPIECES = 64 * BN * 2 / 1024;
  constexpr int LDW = ROLE == 2 ? F_ : 3 * D_;
  constexpr int RBI = ROLE == 2 ? 192 : ROLE == 0 ? 320 : 160;
  constexpr int CPR = RBI / 16;
  constexpr int PPI = 64 * RBI / 1024;
  constexpr int IMGB = 64 * RBI;

  const int tid = threadIdx.x;
  const int wid = __builtin_amdgcn_readfirstlane(tid >> 6);
  const int lane = tid & 63;
  const int g = lane >> 4, l15 = lane & 15;
  const int wm = wid % WM, wn = wid / WM;
  const int m0 = rb * 256;
  const int CSOFF = 2 * wn;

  const char* abase = (const char*)(x + (size_t)m0 * D_);
  const unsigned aoffg = (unsigned)(((wid * 8 + (lane >> 3)) * D_ + (((lane & 7) ^ (lane >> 3)) * 8)) * 2);
  const char* bptr[3];
#pragma unroll
  for (int i = 0; i < 3; ++i) {
    int p = wid + 8 * i;
    if (p >= BPIECES) p = wid;
    const int img = p / PPI, c = (p - img * PPI) * 64 + lane;
    const int rho = c / CPR, chp = c - rho * CPR;
    const int k = ROLE == 1 ? (rho ^ (((rho >> 3) & 1) << 2)) : rho;
    const int ch = ROLE == 1 ? chp : (chp ^ (2 * ((rho >> 3) & 1)));
    const u16* wp;
    if (ROLE == 0) wp = w0 + cgl * 160;
    else if (ROLE == 1) wp = w0 + (img ? 2 * D_ : D_) + cgl * 80;
    else wp = (img ? w1 : w0) + cgl * 96;
    bptr[i] = (const char*)(wp + (size_t)k * LDW + ch * 8);
  }
  auto loadA = [&](int t, int slot, int i) {
    glds16(abase + (size_t)(i * 64 * D_ * 2 + t * 128) + aoffg, smem + slot * K1_ABYTES + (wid + 8 * i) * 1024);
  };
  auto loadB = [&](int t, int slot, int i) {
    if (wid + 8 * i < BPIECES) {
      glds16(bptr[i] + (size_t)t * (64 * LDW * 2), smem + K1_OFF_B + slot * K1_BBYTES + (wid + 8 * i) * 1024);
    }
  };
  auto stageA = [&](int t, int slot) {
#pragma unroll
    for (int i = 0; i < 4; ++i) loadA(t, slot, i);
  };
  auto stageB = [&](int t, int slot) {
#pragma unroll
    for (int i = 0; i < 3; ++i) loadB(t, slot, i);
  };

  const char* pfp;
  if (wid < 4) {
    pfp = abase + (size_t)((64 * q4 + 16 * wid + (lane >> 2)) * D_ * 2 + (lane & 1) * 64);
  } else {
    const int e = lane & 7;
    const u16* wp;
    if (ROLE == 0) wp = w0 + cgl * 160 + min(e, 4) * 32;
    else if (ROLE == 1) wp = (e < 4) ? (w0 + D_ + cgl * 80 + min(e * 32, 78)) : (w0 + 2 * D_ + cgl * 80 + min((e - 4) * 32, 78));
    else wp = (e < 4) ? (w0 + cgl * 96 + min(e, 2) * 32) : (w1 + cgl * 96 + min(e - 4, 2) * 32);
    pfp = (const char*)(wp + (size_t)(8 * rb + 2 * (wid - 4) + (lane >> 5)) * LDW);
  }
  const unsigned pfstep = wid < 4 ? 128u : (unsigned)(64 * LDW * 2);
  auto prefetch = [&](int t) {
    if (t > 9) t = 9;
    __builtin_amdgcn_global_load_lds((const unsigned*)(pfp + (size_t)t * pfstep), (unsigned*)(smem + K1_OFF_DUMMY), 4, 0, 0);
  };
  stageA(0, 0);
  stageB(0, 0);
  stageA(1, 1);
  prefetch(2);
  prefetch(3);

  const int arow = wm * (MT * 16) + l15;
  const unsigned lbase = lds_addr(smem);
  const unsigned aoff0 = lbase + arow * 128 + ((g ^ (lane & 7)) * 16);
  const int bq = l15 >> 2, bp = lane & 3;
  constexpr int NBJ = ROLE == 2 ? 3 : ROLE == 0 ? 5 : 2;
  unsigned bj[NBJ];
  if (ROLE == 1) {
    bj[0] = lbase + K1_OFF_B + (8 * g + bq + 4 * (g & 1)) * RBI + bp * 8;
    bj[1] = lbase + K1_OFF_B + (8 * g + bq + 4 * ((g & 1) ^ 1)) * RBI + bp * 8;
  } else {
#pragma unroll
    for (int j = 0; j < NBJ; ++j) bj[j] = lbase + K1_OFF_B + (8 * g + bq) * RBI + (((wn * NBJ + j) ^ (g & 1)) * 32) + bp * 8;
  }

  f32x4 acc[MT][NT];
#pragma unroll
  for (int i = 0; i < MT; ++i)
#pragma unroll
    for (int j = 0; j < NT; ++j) acc[i][j] = (f32x4){0.f, 0.f, 0.f, 0.f};
  float rs[MT], rq[MT], cs[NT];
#pragma unroll
  for (int i = 0; i < MT; ++i) { rs[i] = 0.f; rq[i] = 0.f; }
#pragma unroll
  for (int j = 0; j < NT; ++j) cs[j] = 0.f;

  bf16x8 xf[2][MT];
  s16x4 wl[2][NT], wh[2][NT];
  auto rd = [&](auto S, unsigned soa, unsigned sob) {
    constexpr int s = S.value;
    const unsigned aa = (aoff0 ^ (s * 64)) + soa;
    sfor<MT>([&](auto I) { xf[s][I.value] = lds_rd128<I.value * 2048>((I.value & 1) ? aa - wn * 2048 : aa + wn * 2048); });
    unsigned bs[NBJ];
#pragma unroll
    for (int j = 0; j < NBJ; ++j) bs[j] = bj[j] + sob;
    sfor<NT>([&](auto J) {
      constexpr int j = J.value;
      if constexpr (ROLE == 1) {
        constexpr int off = (j / 5) * IMGB + s * 32 * RBI + (j % 5) * 32;
        wl[s][j] = tr_rd<off>(bs[0]);
        wh[s][j] = tr_rd<off>(bs[1]);
      } else {
        constexpr int off = (j / NBJ) * IMGB + s * 32 * RBI;
        wl[s][j] = tr_rd<off>(bs[j % NBJ]);
        wh[s][j] = tr_rd<off + 4 * RBI>(bs[j % NBJ]);
      }
    });
  };
  auto mm = [&](auto S, auto LDX, int t, int nslot, bool do_cs) {
    constexpr int s = S.value, LD = LDX.value;
    bf16x8 wf[NT];
#pragma unroll
    for (int j = 0; j < NT; ++j) wf[j] = cat8(wl[s][j], wh[s][j]);
    constexpr int NM = MT * NT;
    constexpr int ND = (WN == 2) ? (MT / 2) * 8 : MT * 8;
    sfor<NM>([&](auto X) {
      constexpr int xi = X.value, i = xi / NT, j = xi % NT;
      if (ROLE == 1) acc[i][j] = __builtin_amdgcn_mfma_f32_16x16x32_bf16(xf[s][i], wf[j], acc[i][j], 0, 0, 0);
      else acc[i][j] = __builtin_amdgcn_mfma_f32_16x16x32_bf16(wf[j], xf[s][i], acc[i][j], 0, 0, 0);
      constexpr int d0 = xi * ND / NM, d1 = (xi + 1) * ND / NM;
      sfor<d1 - d0>([&](auto Y) {
        constexpr int d = d0 + Y.value;
        constexpr int NTL = ND / 8;
        constexpr int q = (d >> 1) % NTL, e = (d >> 1) / NTL;
        constexpr int ii = (WN == 2) ? 2 * q : q;
        unsigned u = __builtin_bit_cast(u32x4, xf[s][ii])[e];
        if (d & 1) rq[q] = dot_self(u, rq[q]); else rs[q] = dot_ones(u, rs[q]);
      });
      constexpr int P = NM / 4;
      if constexpr (LD == 0 && (xi == 1 || xi == 1 + P || xi == 1 + 2 * P)) { if (t + 1 < 10) loadB(t + 1, (t + 1) & 1, (xi - 1) / P); }
      if constexpr (LD == 0 && xi == 1 + 3 * P) { if (t + 2 < 10) loadA(t + 2, nslot, 0); }
      if constexpr (LD == 1 && (xi == 1 || xi == 1 + P || xi == 1 + 2 * P)) { if (t + 2 < 10) loadA(t + 2, nslot, 1 + (xi - 1) / P); }
      if constexpr (LD == 1 && xi == 1 + 3 * P) prefetch(t + 4);
      __builtin_amdgcn_sched_barrier(0);
    });
    if (do_cs) {
#pragma unroll
      for (int j = 0; j < NT; ++j) {
        u32x4 u = __builtin_bit_cast(u32x4, wf[j]);
#pragma unroll
        for (int e = 0; e < 4; ++e) cs[j] = dot_ones(u[e], cs[j]);
      }
    }
  };
  using I0 = std::integral_constant<int, 0>; using I1 = std::integral_constant<int, 1>; using I2 = std::integral_constant<int, 2>;
  constexpr int NRD = MT + 2 * NT;

  int aslot = 0;
  if (wid < 4) {
#pragma nounroll
    for (int t = 0; t < 10; ++t) {
      if (t == 9) WAIT_V(0); else if (t == 0) WAIT_V(6); else WAIT_V(5);
      BAR();
      const int nslot = aslot == 0 ? 2 : aslot - 1;
      const unsigned soa = aslot * K1_ABYTES, sob = (t & 1) * K1_BBYTES;
      aslot = aslot == 2 ? 0 : aslot + 1;
      const bool do_cs = ((t + CSOFF) % WM) == wm;
      rd(I0{}, soa, sob);
      rd(I1{}, soa, sob);
      if (NRD > 15) WAIT_L(15); else WAIT_L(14);
      mm(I0{}, I0{}, t, nslot, do_cs);
      WAIT_L(0);
      mm(I1{}, I1{}, t, nslot, do_cs);
    }
  } else {
    bool prev_cs = (CSOFF % WM) == wm;
    {
      WAIT_V(6);
      BAR();
      rd(I0{}, 0u, 0u);
      stageB(1, 1);
      loadA(2, 2, 0);
      WAIT_L(0);
      rd(I1{}, 0u, 0u);
      mm(I0{}, I1{}, 0, 2, prev_cs);
      WAIT_L(0);
      aslot = 1;
    }
#pragma nounroll
    for (int t = 1; t < 10; ++t) {
      if (t == 9) WAIT_V(0); else WAIT_V(5);
      BAR();
      const int nslot = aslot == 0 ? 2 : aslot - 1;
      const unsigned soa = aslot * K1_ABYTES, sob = (t & 1) * K1_BBYTES;
      aslot = aslot == 2 ? 0 : aslot + 1;
      const bool do_cs = ((t + CSOFF) % WM) == wm;
      rd(I0{}, soa, sob);
      mm(I1{}, I0{}, t, nslot, prev_cs);
      WAIT_L(0);
      rd(I1{}, soa, sob);
      mm(I0{}, I1{}, t, nslot, do_cs);
      WAIT_L(0);
      prev_cs = do_cs;
    }
    mm(I1{}, I2{}, 10, 0, prev_cs);
  }

  float* rsum = (float*)(smem + K1_OFF_RS);
  float* csum = (float*)(smem + K1_OFF_CS);
  float* smean = (float*)(smem + K1_OFF_MEAN);
  float* srsd = smean + 256;
  float* scs = (float*)(smem + K1_OFF_CSF);
#pragma unroll
  for (int q = 0; q < (WN == 2 ? MT / 2 : MT); ++q) {
    int r = wm * (MT * 16) + ((WN == 2) ? ((2 * q) ^ wn) : q) * 16 + l15;
    float2 v = {rs[q], rq[q]};
    *(float2*)(rsum + (g * 256 + r) * 2) = v;
  }
#pragma unroll
  for (int j = 0; j < NT; ++j) {
    int nb = (ROLE == 2) ? ((j < 3) ? (wn * 3 + j) : (6 + wn * 3 + (j - 3))) : (wn * NT + j);
    csum[(wm * 4 + g) * 192 + nb * 16 + l15] = cs[j];
  }
  __syncthreads();
  if (tid < 256) {
    float a = 0.f, b = 0.f;
#pragma unroll
    for (int w = 0; w < 4; ++w) { float2 v = *(const float2*)(rsum + (w * 256 + tid) * 2); a += v.x; b += v.y; }
    float mean = a * (1.0f / D_);
    float var = fmaxf(b * (1.0f / D_) - mean * mean, 0.f);
    float sd = rbf(sqrtf(rbf(rbf(var) + 1e-5f)));
    smean[tid] = rbf(mean);
    srsd[tid] = 1.0f / sd;
  } else if (tid - 256 < BN) {
    int n = tid - 256;
    float a = 0.f;
#pragma unroll
    for (int w = 0; w < WM * 4; ++w) a += csum[w * 192 + n];
    scs[n] = a;
  }
  __syncthreads();

  if (ROLE == 0) {
    constexpr int PITCH = 336;
#pragma unroll
    for (int i = 0; i < MT; ++i) {
      int r = wm * 64 + (i ^ wn) * 16 + l15;
      float mean = smean[r], rsd = srsd[r];
#pragma unroll
      for (int j = 0; j < NT; ++j) {
        int n = (wn * NT + j) * 16 + 4 * g;
        f32x4 c4 = *(const f32x4*)(scs + n);
        float p[4];
#pragma unroll
        for (int e = 0; e < 4; ++e) p[e] = phi_bf(rbf((acc[i][j][e] - mean * c4[e]) * rsd));
        u32x2 o = {pack2(p[0], p[1]), pack2(p[2], p[3])};
        *(u32x2*)(smem + r * PITCH + n * 2) = o;
      }
    }
    __syncthreads();
#pragma unroll
    for (int it = 0; it < 10; ++it) {
      int o = tid + 512 * it, blk = o >> 6, ln = o & 63;
      int rt = blk / 5, un = blk - rt * 5;
      u32x4 v = *(const u32x4*)(smem + (rt * 16 + (ln & 15)) * PITCH + (un * 4 + (ln >> 4)) * 16);
      *(u32x4*)(outp + ((size_t)((rb * 16 + rt) * 20 + cgl * 5 + un) * 64 + ln) * 8) = v;
    }
  } else if (ROLE == 2) {
    constexpr int PITCH = 208;
#pragma unroll
    for (int i = 0; i < MT; ++i) {
      int r = wm * 64 + (i ^ wn) * 16 + l15;
      float mean = smean[r], rsd = srsd[r];
#pragma unroll
      for (int j = 0; j < 3; ++j) {
        int n = (wn * 3 + j) * 16 + 4 * g;
        f32x4 cg4 = *(const f32x4*)(scs + n);
        f32x4 cu4 = *(const f32x4*)(scs + 96 + n);
        float hv[4];
#pragma unroll
        for (int e = 0; e < 4; ++e) {
          float gv = rbf((acc[i][j][e] - mean * cg4[e]) * rsd);
          float uv = rbf((acc[i][j + 3][e] - mean * cu4[e]) * rsd);
          float sg = rbf(__builtin_amdgcn_rcpf(1.0f + __expf(-gv)));
          hv[e] = sg * uv;
        }
        u32x2 o = {pack2(hv[0], hv[1]), pack2(hv[2], hv[3])};
        *(u32x2*)(smem + r * PITCH + n * 2) = o;
      }
    }
    __syncthreads();
#pragma unroll
    for (int it = 0; it < 6; ++it) {
      int o = tid + 512 * it, blk = o >> 6, ln = o & 63;
      int rt = blk / 3, un = blk - rt * 3;
      u32x4 v = *(const u32x4*)(smem + (rt * 16 + (ln & 15)) * PITCH + (un * 4 + (ln >> 4)) * 16);
      *(u32x4*)(outp + ((size_t)((rb * 16 + rt) * 54 + cgl * 3 + un) * 64 + ln) * 8) = v;
    }
  } else {
    bf16x8 fr[10];
    float mean[2][4], rsd[2][4];
#pragma unroll
    for (int i = 0; i < 2; ++i)
#pragma unroll
      for (int e = 0; e < 4; ++e) {
        int r = wid * 32 + i * 16 + 4 * g + e;
        mean[i][e] = smean[r];
        rsd[i][e] = srsd[r];
      }
#pragma unroll
    for (int j = 0; j < 10; ++j) {
      float c = scs[j * 16 + l15];
      float v[8];
#pragma unroll
      for (int i = 0; i < 2; ++i)
#pragma unroll
        for (int e = 0; e < 4; ++e) {
          float t = rbf((acc[i][j][e] - mean[i][e] * c) * rsd[i][e]);
          v[i * 4 + e] = (j < 5) ? phi_bf(t) : t;
        }
      u32x4 u = {pack2(v[0], v[1]), pack2(v[2], v[3]), pack2(v[4], v[5]), pack2(v[6], v[7])};
      fr[j] = __builtin_bit_cast(bf16x8, u);
    }
    float* red = (float*)smem;
    float* myred = red + wid * (4 * KVSZ);
    const bf16x8 ones = {0x3F80, 0x3F80, 0x3F80, 0x3F80, 0x3F80, 0x3F80, 0x3F80, 0x3F80};
#pragma unroll
    for (int i = 0; i < 5; ++i) {
#pragma unroll
      for (int j = 0; j < 5; ++j) {
        if (j < i - 1 || j > i + 1) continue;
        f32x4 z4 = {0.f, 0.f, 0.f, 0.f};
        f32x4 d = __builtin_amdgcn_mfma_f32_16x16x32_bf16(fr[i], fr[5 + j], z4, 0, 0, 0);
        int vc = 16 * j + l15, hv = vc / DH, ev = vc - hv * DH;
#pragma unroll
        for (int e = 0; e < 4; ++e) {
          int kc = 16 * i + 4 * g + e, hk = kc / DH, dk = kc - hk * DH;
          if (hk == hv) myred[hk * KVSZ + dk * DH + ev] = d[e];
        }
      }
      f32x4 z4 = {0.f, 0.f, 0.f, 0.f};
      f32x4 d = __builtin_amdgcn_mfma_f32_16x16x32_bf16(fr[i], ones, z4, 0, 0, 0);
      if (l15 == 0) {
#pragma unroll
        for (int e = 0; e < 4; ++e) {
          int kc = 16 * i + 4 * g + e, hk = kc / DH, dk = kc - hk * DH;
          myred[hk * KVSZ + DH * DH + dk] = d[e];
        }
      }
    }
    __syncthreads();
    for (int o = tid; o < 4 * KVSZ; o += 512) {
      float a = 0.f;
#pragma unroll
      for (int w = 0; w < 8; ++w) a += red[w * (4 * KVSZ) + o];
      kvp[(size_t)(rb * NH + cgl * 4) * KVSZ + o] = a;
    }
  }
}

__global__ __launch_bounds__(512, 2) void k1_kernel(const u16* __restrict__ x, const u16* __restrict__ wqkv,
                                                    const u16* __restrict__ wg, const u16* __restrict__ wu,
                                                    u16* __restrict__ pq, u16* __restrict__ h,
                                                    float* __restrict__ kvp, unsigned* __restrict__ flags) {
  extern __shared__ __attribute__((aligned(16))) char smem[];
  const int bid = blockIdx.x;
  if (bid == 0 && threadIdx.x < 256) flags[threadIdx.x] = 0u;
  const int lin = (bid & 7) * 30 + (bid >> 3);
  const int cg = lin >> 3, rb = lin & 7;
  if (cg < 18) k1_body<2>(x, wg, wu, h, kvp, rb, cg, cg & 3, smem);
  else if (cg < 26) k1_body<1>(x, wqkv, wqkv, pq, kvp, rb, cg - 18, cg & 3, smem);
  else k1_body<0>(x, wqkv, wqkv, pq, kvp, rb, cg - 26, cg & 3, smem);
}

constexpr int K2_BS = 64 * 80 * 2;
constexpr int K2_WREG = 3 * K2_BS;
constexpr int K2_OFF_RD = 4 * K2_WREG;
constexpr int K2_OFF_KV = K2_OFF_RD + 4 * 2048 + 64;
constexpr int K2_OFF_WO = K2_OFF_KV + 1792;
constexpr int K2_OFF_ZS = K2_OFF_WO + 5120;
constexpr int K2_OFF_CNT = K2_OFF_ZS + 4096;
constexpr int K2_LDS = K2_OFF_CNT + 64;

__device__ __forceinline__ unsigned gld32_sc1(const void* sbase, unsigned voff) {
  unsigned r; asm volatile("global_load_dword %0, %1, %2 sc1" : "=&v"(r) : "v"(voff), "s"(sbase) : "memory"); return r;
}

__global__ __launch_bounds__(256, 1) void k2_kernel(const u16* __restrict__ x, const u16* __restrict__ h,
                                                    const u16* __restrict__ wd, const u16* __restrict__ pq,
                                                    const float* __restrict__ kvp, const u16* __restrict__ wo,
                                                    u16* __restrict__ wp, unsigned* __restrict__ flags,
                                                    u16* __restrict__ out) {
  extern __shared__ __attribute__((aligned(16))) char smem[];
  const int bid = blockIdx.x;
  const int lin = (bid & 7) * 32 + (bid >> 3);
  const int rb = lin >> 3, cb = lin & 7;
  const int m0 = rb * 64, n0 = cb * 80;
  const int tid = threadIdx.x;
  const int wid = __builtin_amdgcn_readfirstlane(tid >> 6);
  const int lane = tid & 63;
  const int g = lane >> 4, l15 = lane & 15;
  const int ph = bid >> 3, pj = bid & 7;

  bf16x8 kq[8], zq[8], woq;
  {
    const unsigned ko = (unsigned)(ph * KVSZ + 4 * min(tid, 104)) * 4u;
    sfor<8>([&](auto R) { kq[R.value] = gld128<0>((const char*)kvp + (size_t)R.value * (NH * KVSZ * 4), ko); });
    const int c = min(tid, 199), e = c / 10, ch = c - e * 10;
    woq = gld128<0>(wo, (unsigned)((ph * DH + e) * D_ + pj * 80 + ch * 8) * 2u);
    const int tz = min(lane, 39), hz = tz / 5;
    const unsigned zo = (unsigned)((8 * wid + hz) * KVSZ + DH * DH + 4 * (tz - hz * 5)) * 4u;
    sfor<8>([&](auto R) { zq[R.value] = gld128<0>((const char*)kvp + (size_t)R.value * (NH * KVSZ * 4), zo); });
  }
  if (tid == 0) *(unsigned*)(smem + K2_OFF_CNT) = 0u;

  const unsigned orow = (unsigned)(m0 + wid * 16 + l15);
  unsigned boffs[10];
#pragma unroll
  for (int p = 0; p < 10; ++p) {
    int c = p * 64 + lane;
    int rho = c / 10, ch = c - rho * 10;
    int row = rho ^ (((rho >> 3) & 1) << 2);
    boffs[p] = (unsigned)(row * D_ + n0 + 8 * ch) * 2u;
  }
  char* ring = smem + wid * K2_WREG;
  const char* hb = (const char*)h;
  const char* wdb = (const char*)wd;
  const int rot = wid + (7 * cb) / 2 + 7 * (rb & 3);
  unsigned aoffy[4], poff[4];
#pragma unroll
  for (int i = 0; i < 4; ++i) {
    aoffy[i] = ((unsigned)(rb * 4 + ((i + wid) & 3)) * 54u * 64u + (unsigned)lane) * 16u;
    poff[i] = ((unsigned)(rb * 4 + ((i + wid) & 3)) * 20u * 64u + (unsigned)lane) * 16u;
  }

  bf16x8 af[3][2][4];
  auto loadY = [&](auto SG) {
    constexpr int sg = SG.value, slot = sg % 3;
    const int se = min((rot + 4 * sg) % 28, 26);
#pragma unroll
    for (int p = 0; p < 10; ++p) glds16(wdb + (size_t)se * (64 * D_ * 2) + boffs[p], ring + slot * K2_BS + p * 1024);
    const char* hs = hb + (size_t)se * 2048;
#pragma unroll
    for (int i = 0; i < 4; ++i) af[slot][0][i] = gld128a<0>(hs, aoffy[i]);
#pragma unroll
    for (int i = 0; i < 4; ++i) af[slot][1][i] = gld128a<1024>(hs, aoffy[i]);
    __builtin_amdgcn_sched_barrier(0);
  };
  const char* wpb = (const char*)wp + (size_t)(160 * wid) * D_ * 2;
  auto loadS = [&](auto SG) {
    constexpr int sg = SG.value, slot = (sg + 1) % 3;
#pragma unroll
    for (int p = 0; p < (sg == 2 ? 5 : 10); ++p)
      __builtin_amdgcn_global_load_lds((const unsigned*)(wpb + (size_t)sg * (64 * D_ * 2) + boffs[p]), (unsigned*)(ring + slot * K2_BS + p * 1024), 16, 0, 16);
    __builtin_amdgcn_sched_barrier(0);
  };

  const unsigned lbase = lds_addr(smem);
  const int bq = l15 >> 2, bp = lane & 3;
  const unsigned boffc = lbase + wid * K2_WREG + (8 * g + bq) * 160 + bp * 8;
  const unsigned boff0 = boffc + 640 * (g & 1), boff1 = boffc + 640 * ((g & 1) ^ 1);

  f32x4 accy[4][5], accs[4][5];
#pragma unroll
  for (int i = 0; i < 4; ++i)
#pragma unroll
    for (int j = 0; j < 5; ++j) { accy[i][j] = (f32x4){0.f, 0.f, 0.f, 0.f}; accs[i][j] = (f32x4){0.f, 0.f, 0.f, 0.f}; }

  s16x4 wl[2][5], wh[2][5];
  auto rdB = [&](auto SL, auto S) {
    constexpr int slot = SL.value, s = S.value;
    sfor<5>([&](auto J) {
      constexpr int j = J.value;
      wl[s][j] = tr_rd<slot * K2_BS + s * 5120 + j * 32>(boff0);
      wh[s][j] = tr_rd<slot * K2_BS + s * 5120 + j * 32>(boff1);
    });
  };
  using I0 = std::integral_constant<int, 0>; using I1 = std::integral_constant<int, 1>; using I2 = std::integral_constant<int, 2>;
  using I3 = std::integral_constant<int, 3>; using I4 = std::integral_constant<int, 4>; using I5 = std::integral_constant<int, 5>;
  using I6 = std::integral_constant<int, 6>;
  auto stageY = [&](auto SG) {
    constexpr int slot = SG.value % 3;
    using SL = std::integral_constant<int, slot>;
    if ((rot + 4 * SG.value) % 28 == 27) return;
    rdB(SL{}, I0{});
    rdB(SL{}, I1{});
    WAIT_L(10);
#pragma unroll
    for (int j = 0; j < 5; ++j) {
      bf16x8 wf = cat8(wl[0][j], wh[0][j]);
#pragma unroll
      for (int i = 0; i < 4; ++i) accy[i][j] = __builtin_amdgcn_mfma_f32_16x16x32_bf16(wf, af[slot][0][i], accy[i][j], 0, 0, 0);
    }
    WAIT_L(0);
#pragma unroll
    for (int j = 0; j < 5; ++j) {
      bf16x8 wf = cat8(wl[1][j], wh[1][j]);
#pragma unroll
      for (int i = 0; i < 4; ++i) accy[i][j] = __builtin_amdgcn_mfma_f32_16x16x32_bf16(wf, af[slot][1][i], accy[i][j], 0, 0, 0);
    }
    __builtin_amdgcn_sched_barrier(0);
  };

  loadY(I0{});
  loadY(I1{});
  float* kvs = (float*)(smem + K2_OFF_KV);
  float* zs = (float*)(smem + K2_OFF_ZS + wid * 1024);
  WAIT_V(36);
  {
    f32x4 a = __builtin_bit_cast(f32x4, kq[0]), z = __builtin_bit_cast(f32x4, zq[0]);
#pragma unroll
    for (int r = 1; r < 8; ++r) { a += __builtin_bit_cast(f32x4, kq[r]); z += __builtin_bit_cast(f32x4, zq[r]); }
#pragma unroll
    for (int e = 0; e < 4; ++e) { a[e] = rbf(a[e]); z[e] = rbf(z[e]); }
    if (tid < 105) *(f32x4*)(kvs + 4 * tid) = a;
    if (tid < 200) {
      const int e = tid / 10, ch = tid - e * 10;
      *(bf16x8*)(smem + K2_OFF_WO + (e ^ (((e >> 3) & 1) << 2)) * 160 + ch * 16) = woq;
    }
    if (tid < 120) *(u32x4*)(smem + K2_OFF_WO + 3200 + tid * 16) = (u32x4){0u, 0u, 0u, 0u};
    *(f32x4*)(zs + 4 * lane) = z;
  }
  asm volatile("s_waitcnt lgkmcnt(0)" ::: "memory");
  BAR();
  {
    const unsigned wob = lbase + K2_OFF_WO + (8 * g + bq) * 160 + bp * 8;
    const unsigned wo0 = wob + 640 * (g & 1), wo1 = wob + 640 * ((g & 1) ^ 1);
    s16x4 a0 = tr_rd<0>(wo0 + wid * 32), a1 = tr_rd<0>(wo1 + wid * 32), b0 = tr_rd<128>(wo0), b1 = tr_rd<128>(wo1);
    bf16x8 yk[2];
#pragma unroll
    for (int t = 0; t < 2; ++t) {
      const int d = 16 * t + l15, dc = min(d, 19);
      f32x4 k0 = *(const f32x4*)(kvs + dc * DH + min(8 * g, 16)), k1 = *(const f32x4*)(kvs + dc * DH + min(8 * g + 4, 16));
      const bool v0 = d < DH && g < 3, v1 = d < DH && g < 2;
      u32x4 u = {v0 ? pack2(k0[0], k0[1]) : 0u, v0 ? pack2(k0[2], k0[3]) : 0u, v1 ? pack2(k1[0], k1[1]) : 0u, v1 ? pack2(k1[2], k1[3]) : 0u};
      yk[t] = __builtin_bit_cast(bf16x8, u);
    }
    WAIT_L(0);
    const bf16x8 wa = cat8(a0, a1), wb = cat8(b0, b1);
#pragma unroll
    for (int t = 0; t < 2; ++t) {
      const f32x4 z4 = {0.f, 0.f, 0.f, 0.f};
      f32x4 da = __builtin_amdgcn_mfma_f32_16x16x32_bf16(wa, yk[t], z4, 0, 0, 0);
      f32x4 db = __builtin_amdgcn_mfma_f32_16x16x32_bf16(wb, yk[t], z4, 0, 0, 0);
      const int d = 16 * t + l15;
      u16* dst = wp + (size_t)(ph * DH + d) * D_ + pj * 80 + 4 * g;
      unsigned long long va = (unsigned long long)pack2(da[0], da[1]) | ((unsigned long long)pack2(da[2], da[3]) << 32);
      unsigned long long vb = (unsigned long long)pack2(db[0], db[1]) | ((unsigned long long)pack2(db[2], db[3]) << 32);
      if (d < DH) __hip_atomic_store((unsigned long long*)(dst + 16 * wid), va, __ATOMIC_RELAXED, __HIP_MEMORY_SCOPE_AGENT);
      if (d < DH && g == wid) __hip_atomic_store((unsigned long long*)(dst + 64), vb, __ATOMIC_RELAXED, __HIP_MEMORY_SCOPE_AGENT);
    }
  }
  __builtin_amdgcn_sched_barrier(0);
  bf16x8 pf[5][4];
  {
    const char* pqb = (const char*)pq + (size_t)(5 * wid) * 1024;
    sfor<5>([&](auto U) {
#pragma unroll
      for (int i = 0; i < 4; ++i) pf[U.value][i] = gld128<0>(pqb + U.value * 1024, poff[i]);
    });
  }
  loadY(I2{});
  WAIT_V(60);
  stageY(I0{});
  loadY(I3{});
  WAIT_V(56);
  if (lane == 0) {
    unsigned old;
    asm volatile("ds_add_rtn_u32 %0, %1, %2\n\ts_waitcnt lgkmcnt(0)" : "=&v"(old) : "v"(lbase + K2_OFF_CNT), "v"(1u) : "memory");
    if (old == 3) __hip_atomic_store(flags + pj * 32 + ph, 1u, __ATOMIC_RELAXED, __HIP_MEMORY_SCOPE_AGENT);
  }
  stageY(I1{});
  loadY(I4{});
  WAIT_V(36);
  stageY(I2{});
  unsigned fl = gld32_sc1(flags + cb * 32 + 8 * wid, (unsigned)(lane & 7) * 4u);
  loadY(I5{});
  float* rden = (float*)(smem + K2_OFF_RD) + wid * 512;
  {
    f32x4 dn[4];
#pragma unroll
    for (int i = 0; i < 4; ++i) dn[i] = (f32x4){0.f, 0.f, 0.f, 0.f};
#pragma unroll
    for (int u = 0; u < 5; ++u) {
      f32x4 z0 = *(const f32x4*)(zs + 32 * u + 8 * g), z1 = *(const f32x4*)(zs + 32 * u + 8 * g + 4);
      float zv[8] = {z0[0], z0[1], z0[2], z0[3], z1[0], z1[1], z1[2], z1[3]};
      const int k0 = 32 * u + 8 * g;
      const int h0 = k0 / DH, lim = (h0 + 1) * DH - k0;
      const bool m0_ = (h0 == l15), m1_ = (h0 + 1 == l15);
      u32x4 zm;
#pragma unroll
      for (int e = 0; e < 4; ++e) {
        bool m = (2 * e < lim) ? m0_ : m1_;
        zm[e] = m ? pack2(zv[2 * e], zv[2 * e + 1]) : 0u;
      }
      bf16x8 zz = __builtin_bit_cast(bf16x8, zm);
#pragma unroll
      for (int i = 0; i < 4; ++i) dn[i] = __builtin_amdgcn_mfma_f32_16x16x32_bf16(zz, pf[u][i], dn[i], 0, 0, 0);
    }
    if (g < 2) {
#pragma unroll
      for (int i = 0; i < 4; ++i) {
        f32x4 r;
#pragma unroll
        for (int e = 0; e < 4; ++e) r[e] = __builtin_amdgcn_rcpf(rbf(rbf(dn[i][e]) + 1e-6f));
        *(f32x4*)(rden + (i * 16 + l15) * 8 + 4 * g) = r;
      }
    }
    __builtin_amdgcn_s_waitcnt(0xc07f);
    __builtin_amdgcn_wave_barrier();
#pragma unroll
    for (int u = 0; u < 5; ++u) {
      const int k0 = 32 * u + 8 * g;
      const int h0 = k0 / DH;
      const int lim = (h0 + 1) * DH - k0;
#pragma unroll
      for (int i = 0; i < 4; ++i) {
        const float* rp = rden + (i * 16 + l15) * 8 + h0;
        float r0 = rp[0], r1 = rp[1];
        u32x4 uu = __builtin_bit_cast(u32x4, pf[u][i]);
        u32x4 o;
#pragma unroll
        for (int e = 0; e < 4; ++e) {
          float r = (2 * e < lim) ? r0 : r1;
          o[e] = pack2(bflo(uu[e]) * r, bfhi(uu[e]) * r);
        }
        pf[u][i] = __builtin_bit_cast(bf16x8, o);
      }
    }
    __builtin_amdgcn_s_waitcnt(0xc07f);
    __builtin_amdgcn_sched_barrier(0);
  }
  WAIT_V(37);
  stageY(I3{});
  loadY(I6{});
  WAIT_V(36);
  if (!__all(fl == 1u)) {
    for (;;) {
      unsigned f2 = gld32_sc1(flags + cb * 32 + 8 * wid, (unsigned)(lane & 7) * 4u);
      WAIT_V(0);
      if (__all(f2 == 1u)) break;
      __builtin_amdgcn_s_sleep(2);
    }
  }
  stageY(I4{});
  loadS(I0{});
  WAIT_V(28);
  stageY(I5{});
  loadS(I1{});
  WAIT_V(20);
  stageY(I6{});
  loadS(I2{});
  u32x2 xv[5];
#pragma unroll
  for (int j = 0; j < 5; ++j) xv[j] = *(const u32x2*)(x + (size_t)orow * D_ + n0 + j * 16 + 4 * g);
  __builtin_amdgcn_sched_barrier(0);
  {
    WAIT_V(20);
    rdB(I1{}, I0{});
    rdB(I1{}, I1{});
    WAIT_L(10);
#pragma unroll
    for (int j = 0; j < 5; ++j) {
      bf16x8 wf = cat8(wl[0][j], wh[0][j]);
#pragma unroll
      for (int i = 0; i < 4; ++i) accs[i][j] = __builtin_amdgcn_mfma_f32_16x16x32_bf16(wf, pf[0][i], accs[i][j], 0, 0, 0);
    }
    WAIT_L(0);
#pragma unroll
    for (int j = 0; j < 5; ++j) {
      bf16x8 wf = cat8(wl[1][j], wh[1][j]);
#pragma unroll
      for (int i = 0; i < 4; ++i) accs[i][j] = __builtin_amdgcn_mfma_f32_16x16x32_bf16(wf, pf[1][i], accs[i][j], 0, 0, 0);
    }
    __builtin_amdgcn_sched_barrier(0);
    WAIT_V(10);
    rdB(I2{}, I0{});
    rdB(I2{}, I1{});
    WAIT_L(10);
#pragma unroll
    for (int j = 0; j < 5; ++j) {
      bf16x8 wf = cat8(wl[0][j], wh[0][j]);
#pragma unroll
      for (int i = 0; i < 4; ++i) accs[i][j] = __builtin_amdgcn_mfma_f32_16x16x32_bf16(wf, pf[2][i], accs[i][j], 0, 0, 0);
    }
    WAIT_L(0);
#pragma unroll
    for (int j = 0; j < 5; ++j) {
      bf16x8 wf = cat8(wl[1][j], wh[1][j]);
#pragma unroll
      for (int i = 0; i < 4; ++i) accs[i][j] = __builtin_amdgcn_mfma_f32_16x16x32_bf16(wf, pf[3][i], accs[i][j], 0, 0, 0);
    }
    __builtin_amdgcn_sched_barrier(0);
    WAIT_V(5);
    rdB(I0{}, I0{});
    WAIT_L(0);
#pragma unroll
    for (int j = 0; j < 5; ++j) {
      bf16x8 wf = cat8(wl[0][j], wh[0][j]);
#pragma unroll
      for (int i = 0; i < 4; ++i) accs[i][j] = __builtin_amdgcn_mfma_f32_16x16x32_bf16(wf, pf[4][i], accs[i][j], 0, 0, 0);
    }
  }
#pragma unroll
  for (int i = 1; i < 4; ++i)
#pragma unroll
    for (int j = 0; j < 5; ++j) {
      *(f32x4*)(ring + (i - 1) * K2_BS + j * 1024 + lane * 16) = accy[i][j];
      *(f32x4*)(ring + (i - 1) * K2_BS + (5 + j) * 1024 + lane * 16) = accs[i][j];
    }
  __syncthreads();
#pragma unroll
  for (int i = 1; i < 4; ++i) {
    const char* src = smem + ((wid - i) & 3) * K2_WREG + (i - 1) * K2_BS + lane * 16;
#pragma unroll
    for (int j = 0; j < 5; ++j) {
      f32x4 a = *(const f32x4*)(src + j * 1024);
      f32x4 b = *(const f32x4*)(src + (5 + j) * 1024);
      accy[0][j] += a;
      accs[0][j] += b;
    }
  }
#pragma unroll
  for (int j = 0; j < 5; ++j) {
    size_t o = (size_t)orow * D_ + n0 + j * 16 + 4 * g;
    float xs[4] = {bflo(xv[j][0]), bfhi(xv[j][0]), bflo(xv[j][1]), bfhi(xv[j][1])};
    float r[4];
#pragma unroll
    for (int e = 0; e < 4; ++e) {
      float y = rbf(accy[0][j][e]);
      float gt = rbf(y * __builtin_amdgcn_rcpf(1.0f + __expf(-y)));
      float sv = rbf(accs[0][j][e]);
      r[e] = xs[e] + rbf(sv * gt);
    }
    u32x2 ov = {pack2(r[0], r[1]), pack2(r[2], r[3])};
    *(u32x2*)(out + o) = ov;
  }
}

extern "C" void kernel_launch(void* const* d_in, const int* in_sizes, int n_in, void* d_out, int out_size,
                              void* d_ws, size_t ws_size, hipStream_t stream) {
  const u16* x = (const u16*)d_in[0];
  const u16* wqkv = (const u16*)d_in[1];
  const u16* wo = (const u16*)d_in[2];
  const u16* wg = (const u16*)d_in[4];
  const u16* wu = (const u16*)d_in[5];
  const u16* wd = (const u16*)d_in[6];
  char* ws = (char*)d_ws;
  u16* pq = (u16*)(ws);
  u16* h = (u16*)(ws + (4 << 20));
  float* kvp = (float*)(ws + (12 << 20));
  u16* wp = (u16*)(ws + (13 << 20));
  unsigned* flags = (unsigned*)(ws + (14 << 20));
  hipFuncSetAttribute((const void*)k1_kernel, hipFuncAttributeMaxDynamicSharedMemorySize, K1_LDS);
  hipFuncSetAttribute((const void*)k2_kernel, hipFuncAttributeMaxDynamicSharedMemorySize, K2_LDS);
  k1_kernel<<<240, 512, K1_LDS, stream>>>(x, wqkv, wg, wu, pq, h, kvp, flags);
  k2_kernel<<<256, 256, K2_LDS, stream>>>(x, h, wd, pq, kvp, wo, wp, flags, (u16*)d_out);
}
```

```cpp
#include <hip/hip_runtime.h>
#include <hip/hip_bf16.h>
#include <stdint.h>
#include <utility>

typedef unsigned short u16;
typedef __attribute__((ext_vector_type(8))) short bf16x8;
typedef __attribute__((ext_vector_type(4))) short s16x4;
typedef __attribute__((ext_vector_type(4))) float f32x4;
typedef __attribute__((ext_vector_type(2))) __bf16 bf2;
typedef __attribute__((ext_vector_type(4))) unsigned u32x4;
typedef __attribute__((ext_vector_type(2))) unsigned u32x2;
#define LAS __attribute__((address_space(3)))

constexpr int S_ = 2048, D_ = 640, F_ = 1728, NH = 32, DH = 20;
constexpr int KVSZ = DH * DH + DH;

__device__ __forceinline__ unsigned pack2(float a, float b) {
  bf2 v = {(__bf16)a, (__bf16)b};
  return __builtin_bit_cast(unsigned, v);
}
__device__ __forceinline__ float rbf(float a) { return (float)(__bf16)a; }
__device__ __forceinline__ float bflo(unsigned u) { return __uint_as_float(u << 16); }
__device__ __forceinline__ float bfhi(unsigned u) { return __uint_as_float(u & 0xffff0000u); }
__device__ __forceinline__ float dot_ones(unsigned a, float c) {
  bf2 one = {(__bf16)1.0f, (__bf16)1.0f};
  return __builtin_amdgcn_fdot2_f32_bf16(__builtin_bit_cast(bf2, a), one, c, false);
}
__device__ __forceinline__ float dot_self(unsigned a, float c) {
  bf2 v = __builtin_bit_cast(bf2, a);
  return __builtin_amdgcn_fdot2_f32_bf16(v, v, c, false);
}
__device__ __forceinline__ void glds16(const void* g, void* l) {
  __builtin_amdgcn_global_load_lds((const unsigned*)g, (unsigned*)l, 16, 0, 0);
}
template <class F, int... I> __device__ __forceinline__ void sfor_impl(F&& f, std::integer_sequence<int, I...>) {
  (f(std::integral_constant<int, I>{}), ...);
}
template <int N, class F> __device__ __forceinline__ void sfor(F&& f) { sfor_impl(f, std::make_integer_sequence<int, N>{}); }
#define WAIT_V(n) asm volatile("s_waitcnt vmcnt(" #n ")" ::: "memory")
#define BAR() __builtin_amdgcn_s_barrier()
template <int OFF> __device__ __forceinline__ s16x4 tr_rd(unsigned a) {
  s16x4 r; asm volatile("ds_read_b64_tr_b16 %0, %1 offset:%2" : "=&v"(r) : "v"(a), "i"(OFF) : "memory"); return r;
}
template <int OFF> __device__ __forceinline__ bf16x8 lds_rd128(unsigned a) {
  bf16x8 r; asm volatile("ds_read_b128 %0, %1 offset:%2" : "=&v"(r) : "v"(a), "i"(OFF) : "memory"); return r;
}
template <int OFF> __device__ __forceinline__ bf16x8 gld128(const void* sbase, unsigned voff) {
  bf16x8 r; asm volatile("global_load_dwordx4 %0, %1, %2 offset:%3" : "=&v"(r) : "v"(voff), "s"(sbase), "i"(OFF) : "memory"); return r;
}
template <int OFF> __device__ __forceinline__ bf16x8 gld128a(const void* sbase, unsigned voff) {
  bf16x8 r; asm volatile("global_load_dwordx4 %0, %1, %2 offset:%3" : "=&a"(r) : "v"(voff), "s"(sbase), "i"(OFF) : "memory"); return r;
}
__device__ __forceinline__ bf16x8 cat8(s16x4 a, s16x4 b) { return __builtin_shufflevector(a, b, 0, 1, 2, 3, 4, 5, 6, 7); }
#define WAIT_L(n) do { asm volatile("s_waitcnt lgkmcnt(" #n ")" ::: "memory"); __builtin_amdgcn_sched_barrier(0); } while (0)
__device__ __forceinline__ unsigned lds_addr(const void* p) { return (unsigned)(size_t)(LAS const char*)p; }
__device__ __forceinline__ float phi_bf(float t) {
  float p = t > 0.f ? rbf(t + 1.0f) : rbf(__expf(t));
  return rbf(p + 1e-6f);
}

constexpr int K1_ABYTES = 256 * 64 * 2;
constexpr int K1_BBYTES = 64 * 192 * 2;
constexpr int K1_OFF_B = 3 * K1_ABYTES;
constexpr int K1_OFF_MISC = K1_OFF_B + 2 * K1_BBYTES;
constexpr int K1_OFF_DUMMY = K1_OFF_MISC;
constexpr int K1_OFF_MEAN = K1_OFF_DUMMY + 1024;
constexpr int K1_OFF_CSF = K1_OFF_MEAN + 2048;
constexpr int K1_LDS = K1_OFF_CSF + 768;
constexpr int K1_OFF_RS = K1_ABYTES;
constexpr int K1_OFF_CS = 2 * K1_ABYTES;

template <int ROLE>
__device__ __forceinline__ void k1_body(const u16* __restrict__ x, const u16* __restrict__ w0,
                                        const u16* __restrict__ w1, u16* __restrict__ outp,
                                        float* __restrict__ kvp, int rb, int cgl, const char* __restrict__ wdp,
                                        const char* __restrict__ wop, char* smem) {
  constexpr int BN = ROLE == 2 ? 192 : 160;
  constexpr int NB = BN / 16;
  constexpr int WM = ROLE == 1 ? 8 : 4;
  constexpr int WN = 8 / WM;
  constexpr int MT = 16 / WM;
  constexpr int NT = NB / WN;
  constexpr int BPIECES = 64 * BN * 2 / 1024;
  constexpr int LDW = ROLE == 2 ? F_ : 3 * D_;
  constexpr int RBI = ROLE == 2 ? 192 : ROLE == 0 ? 320 : 160;
  constexpr int CPR = RBI / 16;
  constexpr int PPI = 64 * RBI / 1024;
  constexpr int IMGB = 64 * RBI;

  const int tid = threadIdx.x;
  const int wid = __builtin_amdgcn_readfirstlane(tid >> 6);
  const int lane = tid & 63;
  const int g = lane >> 4, l15 = lane & 15;
  const int wm = wid % WM, wn = wid / WM;
  const int m0 = rb * 256;
  const int CSOFF = 2 * wn;

  const char* abase = (const char*)(x + (size_t)m0 * D_);
  const unsigned aoffg = (unsigned)(((wid * 8 + (lane >> 3)) * D_ + (((lane & 7) ^ (lane >> 3)) * 8)) * 2);
  const char* bptr[3];
#pragma unroll
  for (int i = 0; i < 3; ++i) {
    int p = wid + 8 * i;
    if (p >= BPIECES) p = wid;
    const int img = p / PPI, c = (p - img * PPI) * 64 + lane;
    const int rho = c / CPR, chp = c - rho * CPR;
    const int k = ROLE == 1 ? (rho ^ (((rho >> 3) & 1) << 2)) : rho;
    const int ch = ROLE == 1 ? chp : (chp ^ (2 * ((rho >> 3) & 1)));
    const u16* wp;
    if (ROLE == 0) wp = w0 + cgl * 160;
    else if (ROLE == 1) wp = w0 + (img ? 2 * D_ : D_) + cgl * 80;
    else wp = (img ? w1 : w0) + cgl * 96;
    bptr[i] = (const char*)(wp + (size_t)k * LDW + ch * 8);
  }
  auto loadA = [&](int t, int slot, int i) {
    glds16(abase + (size_t)(i * 64 * D_ * 2 + t * 128) + aoffg, smem + slot * K1_ABYTES + (wid + 8 * i) * 1024);
  };
  auto loadB = [&](int t, int slot, int i) {
    if (wid + 8 * i < BPIECES) {
      glds16(bptr[i] + (size_t)t * (64 * LDW * 2), smem + K1_OFF_B + slot * K1_BBYTES + (wid + 8 * i) * 1024);
    }
  };
  auto stageA = [&](int t, int slot) {
#pragma unroll
    for (int i = 0; i < 4; ++i) loadA(t, slot, i);
  };
  auto stageB = [&](int t, int slot) {
#pragma unroll
    for (int i = 0; i < 3; ++i) loadB(t, slot, i);
  };

  const char* pfw = wdp + (size_t)((blockIdx.x >> 3) * 72 + wid * 9) * 1024 + lane * 16;
  const char* pfo = wop + (size_t)((blockIdx.x * 8 + wid) % 800) * 1024 + lane * 16;
  auto prefetch = [&](int i) {
    glds16(i < 9 ? pfw + (size_t)i * 1024 : pfo, smem + K1_OFF_DUMMY);
  };
  stageA(0, 0);
  stageB(0, 0);
  stageA(1, 1);

  const int arow = wm * (MT * 16) + l15;
  const unsigned lbase = lds_addr(smem);
  const unsigned aoff0 = lbase + arow * 128 + ((g ^ (lane & 7)) * 16);
  const int bq = l15 >> 2, bp = lane & 3;
  constexpr int NBJ = ROLE == 2 ? 3 : ROLE == 0 ? 5 : 2;
  unsigned bj[NBJ];
  if (ROLE == 1) {
    bj[0] = lbase + K1_OFF_B + (8 * g + bq + 4 * (g & 1)) * RBI + bp * 8;
    bj[1] = lbase + K1_OFF_B + (8 * g + bq + 4 * ((g & 1) ^ 1)) * RBI + bp * 8;
  } else {
#pragma unroll
    for (int j = 0; j < NBJ; ++j) bj[j] = lbase + K1_OFF_B + (8 * g + bq) * RBI + (((wn * NBJ + j) ^ (g & 1)) * 32) + bp * 8;
  }

  f32x4 acc[MT][NT];
#pragma unroll
  for (int i = 0; i < MT; ++i)
#pragma unroll
    for (int j = 0; j < NT; ++j) acc[i][j] = (f32x4){0.f, 0.f, 0.f, 0.f};
  float rs[MT], rq[MT], cs[NT];
#pragma unroll
  for (int i = 0; i < MT; ++i) { rs[i] = 0.f; rq[i] = 0.f; }
#pragma unroll
  for (int j = 0; j < NT; ++j) cs[j] = 0.f;

  bf16x8 xf[2][MT];
  s16x4 wl[2][NT], wh[2][NT];
  auto rd = [&](auto S, unsigned soa, unsigned sob) {
    constexpr int s = S.value;
    const unsigned aa = (aoff0 ^ (s * 64)) + soa;
    sfor<MT>([&](auto I) { xf[s][I.value] = lds_rd128<I.value * 2048>((I.value & 1) ? aa - wn * 2048 : aa + wn * 2048); });
    unsigned bs[NBJ];
#pragma unroll
    for (int j = 0; j < NBJ; ++j) bs[j] = bj[j] + sob;
    sfor<NT>([&](auto J) {
      constexpr int j = J.value;
      if constexpr (ROLE == 1) {
        constexpr int off = (j / 5) * IMGB + s * 32 * RBI + (j % 5) * 32;
        wl[s][j] = tr_rd<off>(bs[0]);
        wh[s][j] = tr_rd<off>(bs[1]);
      } else {
        constexpr int off = (j / NBJ) * IMGB + s * 32 * RBI;
        wl[s][j] = tr_rd<off>(bs[j % NBJ]);
        wh[s][j] = tr_rd<off + 4 * RBI>(bs[j % NBJ]);
      }
    });
  };
  auto rd1 = [&](auto S, auto R, unsigned aa, const unsigned* bs) {
    constexpr int s = S.value, r = R.value;
    if constexpr (r < MT) {
      xf[s][r] = lds_rd128<r * 2048>((r & 1) ? aa - wn * 2048 : aa + wn * 2048);
    } else {
      constexpr int j = (r - MT) / 2, hi = (r - MT) & 1;
      if constexpr (ROLE == 1) {
        constexpr int off = (j / 5) * IMGB + s * 32 * RBI + (j % 5) * 32;
        if constexpr (hi) wh[s][j] = tr_rd<off>(bs[1]); else wl[s][j] = tr_rd<off>(bs[0]);
      } else {
        constexpr int off = (j / NBJ) * IMGB + s * 32 * RBI;
        if constexpr (hi) wh[s][j] = tr_rd<off + 4 * RBI>(bs[j % NBJ]); else wl[s][j] = tr_rd<off>(bs[j % NBJ]);
      }
    }
  };
  auto mm = [&](auto S, auto LDX, int t, int nslot, bool do_cs, auto RSX, unsigned soa, unsigned sob) {
    constexpr int s = S.value, LD = LDX.value, RS = RSX.value;
    constexpr int NRDX = MT + 2 * NT;
    const unsigned aa = (aoff0 ^ ((RS & 1) * 64)) + soa;
    unsigned bs[NBJ];
#pragma unroll
    for (int j = 0; j < NBJ; ++j) bs[j] = bj[j] + sob;
    bf16x8 wf[NT];
#pragma unroll
    for (int j = 0; j < NT; ++j) wf[j] = cat8(wl[s][j], wh[s][j]);
    constexpr int NM = MT * NT;
    constexpr int ND = (WN == 2) ? (MT / 2) * 8 : MT * 8;
    sfor<NM>([&](auto X) {
      constexpr int xi = X.value, i = xi / NT, j = xi % NT;
      if (ROLE == 1) acc[i][j] = __builtin_amdgcn_mfma_f32_16x16x32_bf16(xf[s][i], wf[j], acc[i][j], 0, 0, 0);
      else acc[i][j] = __builtin_amdgcn_mfma_f32_16x16x32_bf16(wf[j], xf[s][i], acc[i][j], 0, 0, 0);
      constexpr int d0 = xi * ND / NM, d1 = (xi + 1) * ND / NM;
      sfor<d1 - d0>([&](auto Y) {
        constexpr int d = d0 + Y.value;
        constexpr int NTL = ND / 8;
        constexpr int q = (d >> 1) % NTL, e = (d >> 1) / NTL;
        constexpr int ii = (WN == 2) ? 2 * q : q;
        unsigned u = __builtin_bit_cast(u32x4, xf[s][ii])[e];
        if (d & 1) rq[q] = dot_self(u, rq[q]); else rs[q] = dot_ones(u, rs[q]);
      });
      if constexpr (RS < 2) {
        constexpr int NMR = (NM * 3) / 4;
        constexpr int r0 = xi < NMR ? xi * NRDX / NMR : NRDX, r1 = xi + 1 < NMR ? (xi + 1) * NRDX / NMR : NRDX;
        sfor<r1 - r0>([&](auto Y) { rd1(std::integral_constant<int, RS & 1>{}, std::integral_constant<int, r0 + Y.value>{}, aa, bs); });
      }
      constexpr int P = NM / 4;
      if constexpr (LD == 0 && (xi == 1 || xi == 1 + P || xi == 1 + 2 * P)) { if (t + 1 < 10) loadB(t + 1, (t + 1) & 1, (xi - 1) / P); else prefetch(4 + (xi - 1) / P); }
      if constexpr (LD == 0 && xi == 1 + 3 * P) { if (t + 2 < 10) loadA(t + 2, nslot, 0); else prefetch(t == 8 ? 0 : 7); }
      if constexpr (LD == 1 && (xi == 1 || xi == 1 + P || xi == 1 + 2 * P)) {
        if (t + 2 < 10) loadA(t + 2, nslot, 1 + (xi - 1) / P);
        else if (t == 8) prefetch(1 + (xi - 1) / P);
        else if ((xi - 1) / P < 2) prefetch(8 + (xi - 1) / P);
      }
      __builtin_amdgcn_sched_barrier(0);
    });
    if (do_cs) {
#pragma unroll
      for (int j = 0; j < NT; ++j) {
        u32x4 u = __builtin_bit_cast(u32x4, wf[j]);
#pragma unroll
        for (int e = 0; e < 4; ++e) cs[j] = dot_ones(u[e], cs[j]);
      }
    }
  };
  using I0 = std::integral_constant<int, 0>; using I1 = std::integral_constant<int, 1>; using I2 = std::integral_constant<int, 2>;
  constexpr int NRD = MT + 2 * NT;

  int aslot = 0;
  {
    bool prev_cs = (CSOFF % WM) == wm;
    {
      WAIT_V(4);
      BAR();
      rd(I0{}, 0u, 0u);
      stageB(1, 1);
      loadA(2, 2, 0);
      WAIT_L(0);
      mm(I0{}, I1{}, 0, 2, prev_cs, I1{}, 0u, 0u);
      WAIT_L(0);
      aslot = 1;
    }
#pragma nounroll
    for (int t = 1; t < 10; ++t) {
      WAIT_V(4);
      BAR();
      const int nslot = aslot == 0 ? 2 : aslot - 1;
      const unsigned soa = aslot * K1_ABYTES, sob = (t & 1) * K1_BBYTES;
      aslot = aslot == 2 ? 0 : aslot + 1;
      const bool do_cs = ((t + CSOFF) % WM) == wm;
      mm(I1{}, I0{}, t, nslot, prev_cs, I0{}, soa, sob);
      WAIT_L(0);
      mm(I0{}, I1{}, t, nslot, do_cs, I1{}, soa, sob);
      WAIT_L(0);
      prev_cs = do_cs;
    }
    mm(I1{}, I2{}, 10, 0, prev_cs, I2{}, 0u, 0u);
  }

  float* rsum = (float*)(smem + K1_OFF_RS);
  float* csum = (float*)(smem + K1_OFF_CS);
  float* smean = (float*)(smem + K1_OFF_MEAN);
  float* srsd = smean + 256;
  float* scs = (float*)(smem + K1_OFF_CSF);
#pragma unroll
  for (int q = 0; q < (WN == 2 ? MT / 2 : MT); ++q) {
    int r = wm * (MT * 16) + ((WN == 2) ? ((2 * q) ^ wn) : q) * 16 + l15;
    float2 v = {rs[q], rq[q]};
    *(float2*)(rsum + (g * 256 + r) * 2) = v;
  }
#pragma unroll
  for (int j = 0; j < NT; ++j) {
    int nb = (ROLE == 2) ? ((j < 3) ? (wn * 3 + j) : (6 + wn * 3 + (j - 3))) : (wn * NT + j);
    csum[(wm * 4 + g) * 192 + nb * 16 + l15] = cs[j];
  }
  WAIT_L(0);
  BAR();
  if (tid < 256) {
    float a = 0.f, b = 0.f;
#pragma unroll
    for (int w = 0; w < 4; ++w) { float2 v = *(const float2*)(rsum + (w * 256 + tid) * 2); a += v.x; b += v.y; }
    float mean = a * (1.0f / D_);
    float var = fmaxf(b * (1.0f / D_) - mean * mean, 0.f);
    float sd = rbf(sqrtf(rbf(rbf(var) + 1e-5f)));
    smean[tid] = rbf(mean);
    srsd[tid] = 1.0f / sd;
  } else if (tid - 256 < BN) {
    int n = tid - 256;
    float a = 0.f;
#pragma unroll
    for (int w = 0; w < WM * 4; ++w) a += csum[w * 192 + n];
    scs[n] = a;
  }
  WAIT_L(0);
  BAR();

  if (ROLE == 0) {
    constexpr int PITCH = 336;
#pragma unroll
    for (int i = 0; i < MT; ++i) {
      int r = wm * 64 + (i ^ wn) * 16 + l15;
      float mean = smean[r], rsd = srsd[r];
#pragma unroll
      for (int j = 0; j < NT; ++j) {
        int n = (wn * NT + j) * 16 + 4 * g;
        f32x4 c4 = *(const f32x4*)(scs + n);
        float p[4];
#pragma unroll
        for (int e = 0; e < 4; ++e) p[e] = phi_bf(rbf((acc[i][j][e] - mean * c4[e]) * rsd));
        u32x2 o = {pack2(p[0], p[1]), pack2(p[2], p[3])};
        *(u32x2*)(smem + r * PITCH + n * 2) = o;
      }
    }
    __syncthreads();
#pragma unroll
    for (int it = 0; it < 10; ++it) {
      int o = tid + 512 * it, blk = o >> 6, ln = o & 63;
      int rt = blk / 5, un = blk - rt * 5;
      u32x4 v = *(const u32x4*)(smem + (rt * 16 + (ln & 15)) * PITCH + (un * 4 + (ln >> 4)) * 16);
      *(u32x4*)(outp + ((size_t)((rb * 16 + rt) * 20 + cgl * 5 + un) * 64 + ln) * 8) = v;
    }
  } else if (ROLE == 2) {
    constexpr int PITCH = 208;
#pragma unroll
    for (int i = 0; i < MT; ++i) {
      int r = wm * 64 + (i ^ wn) * 16 + l15;
      float mean = smean[r], rsd = srsd[r];
#pragma unroll
      for (int j = 0; j < 3; ++j) {
        int n = (wn * 3 + j) * 16 + 4 * g;
        f32x4 cg4 = *(const f32x4*)(scs + n);
        f32x4 cu4 = *(const f32x4*)(scs + 96 + n);
        float hv[4];
#pragma unroll
        for (int e = 0; e < 4; ++e) {
          float gv = rbf((acc[i][j][e] - mean * cg4[e]) * rsd);
          float uv = rbf((acc[i][j + 3][e] - mean * cu4[e]) * rsd);
          float sg = rbf(__builtin_amdgcn_rcpf(1.0f + __expf(-gv)));
          hv[e] = sg * uv;
        }
        u32x2 o = {pack2(hv[0], hv[1]), pack2(hv[2], hv[3])};
        *(u32x2*)(smem + r * PITCH + n * 2) = o;
      }
    }
    __syncthreads();
#pragma unroll
    for (int it = 0; it < 6; ++it) {
      int o = tid + 512 * it, blk = o >> 6, ln = o & 63;
      int rt = blk / 3, un = blk - rt * 3;
      u32x4 v = *(const u32x4*)(smem + (rt * 16 + (ln & 15)) * PITCH + (un * 4 + (ln >> 4)) * 16);
      *(u32x4*)(outp + ((size_t)((rb * 16 + rt) * 54 + cgl * 3 + un) * 64 + ln) * 8) = v;
    }
  } else {
    bf16x8 fr[10];
    float mean[2][4], rsd[2][4];
#pragma unroll
    for (int i = 0; i < 2; ++i)
#pragma unroll
      for (int e = 0; e < 4; ++e) {
        int r = wid * 32 + i * 16 + 4 * g + e;
        mean[i][e] = smean[r];
        rsd[i][e] = srsd[r];
      }
#pragma unroll
    for (int j = 0; j < 10; ++j) {
      float c = scs[j * 16 + l15];
      float v[8];
#pragma unroll
      for (int i = 0; i < 2; ++i)
#pragma unroll
        for (int e = 0; e < 4; ++e) {
          float t = rbf((acc[i][j][e] - mean[i][e] * c) * rsd[i][e]);
          v[i * 4 + e] = (j < 5) ? phi_bf(t) : t;
        }
      u32x4 u = {pack2(v[0], v[1]), pack2(v[2], v[3]), pack2(v[4], v[5]), pack2(v[6], v[7])};
      fr[j] = __builtin_bit_cast(bf16x8, u);
    }
    float* red = (float*)smem;
    float* myred = red + wid * (4 * KVSZ);
    const bf16x8 ones = {0x3F80, 0x3F80, 0x3F80, 0x3F80, 0x3F80, 0x3F80, 0x3F80, 0x3F80};
#pragma unroll
    for (int i = 0; i < 5; ++i) {
#pragma unroll
      for (int j = 0; j < 5; ++j) {
        if (j < i - 1 || j > i + 1) continue;
        f32x4 z4 = {0.f, 0.f, 0.f, 0.f};
        f32x4 d = __builtin_amdgcn_mfma_f32_16x16x32_bf16(fr[i], fr[5 + j], z4, 0, 0, 0);
        int vc = 16 * j + l15, hv = vc / DH, ev = vc - hv * DH;
#pragma unroll
        for (int e = 0; e < 4; ++e) {
          int kc = 16 * i + 4 * g + e, hk = kc / DH, dk = kc - hk * DH;
          if (hk == hv) myred[hk * KVSZ + dk * DH + ev] = d[e];
        }
      }
      f32x4 z4 = {0.f, 0.f, 0.f, 0.f};
      f32x4 d = __builtin_amdgcn_mfma_f32_16x16x32_bf16(fr[i], ones, z4, 0, 0, 0);
      if (l15 == 0) {
#pragma unroll
        for (int e = 0; e < 4; ++e) {
          int kc = 16 * i + 4 * g + e, hk = kc / DH, dk = kc - hk * DH;
          myred[hk * KVSZ + DH * DH + dk] = d[e];
        }
      }
    }
    __syncthreads();
    for (int o = tid; o < 4 * KVSZ; o += 512) {
      float a = 0.f;
#pragma unroll
      for (int w = 0; w < 8; ++w) a += red[w * (4 * KVSZ) + o];
      kvp[(size_t)(rb * NH + cgl * 4) * KVSZ + o] = a;
    }
  }
}

__global__ __launch_bounds__(512, 2) void k1_kernel(const u16* __restrict__ x, const u16* __restrict__ wqkv,
                                                    const u16* __restrict__ wg, const u16* __restrict__ wu,
                                                    u16* __restrict__ pq, u16* __restrict__ h,
                                                    float* __restrict__ kvp, unsigned* __restrict__ flags,
                                                    const u16* __restrict__ wd, const u16* __restrict__ wo) {
  extern __shared__ __attribute__((aligned(16))) char smem[];
  const int bid = blockIdx.x;
  if (bid == 0 && threadIdx.x < 256) flags[threadIdx.x] = 0u;
  const int lin = (bid & 7) * 30 + (bid >> 3);
  const int cg = lin >> 3, rb = lin & 7;
  if (cg < 18) k1_body<2>(x, wg, wu, h, kvp, rb, cg, (const char*)wd, (const char*)wo, smem);
  else if (cg < 26) k1_body<1>(x, wqkv, wqkv, pq, kvp, rb, cg - 18, (const char*)wd, (const char*)wo, smem);
  else k1_body<0>(x, wqkv, wqkv, pq, kvp, rb, cg - 26, (const char*)wd, (const char*)wo, smem);
}

constexpr int K2_BS = 64 * 80 * 2;
constexpr int K2_WREG = 3 * K2_BS;
constexpr int K2_OFF_RD = 4 * K2_WREG;
constexpr int K2_OFF_KV = K2_OFF_RD + 4 * 2048 + 64;
constexpr int K2_OFF_WO = K2_OFF_KV + 1792;
constexpr int K2_OFF_ZS = K2_OFF_WO + 5120;
constexpr int K2_OFF_CNT = K2_OFF_ZS + 4096;
constexpr int K2_LDS = K2_OFF_CNT + 64;

__device__ __forceinline__ unsigned gld32_sc1(const void* sbase, unsigned voff) {
  unsigned r; asm volatile("global_load_dword %0, %1, %2 sc1" : "=&v"(r) : "v"(voff), "s"(sbase) : "memory"); return r;
}

__global__ __launch_bounds__(256, 1) void k2_kernel(const u16* __restrict__ x, const u16* __restrict__ h,
                                                    const u16* __restrict__ wd, const u16* __restrict__ pq,
                                                    const float* __restrict__ kvp, const u16* __restrict__ wo,
                                                    u16* __restrict__ wp, unsigned* __restrict__ flags,
                                                    u16* __restrict__ out) {
  extern __shared__ __attribute__((aligned(16))) char smem[];
  const int bid = blockIdx.x;
  const int lin = (bid & 7) * 32 + (bid >> 3);
  const int rb = lin >> 3, cb = lin & 7;
  const int m0 = rb * 64, n0 = cb * 80;
  const int tid = threadIdx.x;
  const int wid = __builtin_amdgcn_readfirstlane(tid >> 6);
  const int lane = tid & 63;
  const int g = lane >> 4, l15 = lane & 15;
  const int ph = bid >> 3, pj = bid & 7;

  bf16x8 kq[8], zq[8], woq;
  {
    const unsigned ko = (unsigned)(ph * KVSZ + 4 * min(tid, 104)) * 4u;
    sfor<8>([&](auto R) { kq[R.value] = gld128<0>((const char*)kvp + (size_t)R.value * (NH * KVSZ * 4), ko); });
    const int c = min(tid, 199), e = c / 10, ch = c - e * 10;
    woq = gld128<0>(wo, (unsigned)((ph * DH + e) * D_ + pj * 80 + ch * 8) * 2u);
    const int tz = min(lane, 39), hz = tz / 5;
    const unsigned zo = (unsigned)((8 * wid + hz) * KVSZ + DH * DH + 4 * (tz - hz * 5)) * 4u;
    sfor<8>([&](auto R) { zq[R.value] = gld128<0>((const char*)kvp + (size_t)R.value * (NH * KVSZ * 4), zo); });
  }
  if (tid == 0) *(unsigned*)(smem + K2_OFF_CNT) = 0u;

  const unsigned orow = (unsigned)(m0 + wid * 16 + l15);
  unsigned boffs[10];
#pragma unroll
  for (int p = 0; p < 10; ++p) {
    int c = p * 64 + lane;
    int rho = c / 10, ch = c - rho * 10;
    int row = rho ^ (((rho >> 3) & 1) << 2);
    boffs[p] = (unsigned)(row * D_ + n0 + 8 * ch) * 2u;
  }
  char* ring = smem + wid * K2_WREG;
  const char* hb = (const char*)h;
  const char* wdb = (const char*)wd;
  const int rot = wid + (7 * cb) / 2 + 7 * (rb & 3);
  unsigned aoffy[4], poff[4];
#pragma unroll
  for (int i = 0; i < 4; ++i) {
    aoffy[i] = ((unsigned)(rb * 4 + ((i + wid) & 3)) * 54u * 64u + (unsigned)lane) * 16u;
    poff[i] = ((unsigned)(rb * 4 + ((i + wid) & 3)) * 20u * 64u + (unsigned)lane) * 16u;
  }

  bf16x8 af[3][2][4];
  auto loadY = [&](auto SG) {
    constexpr int sg = SG.value, slot = sg % 3;
    const int se = min((rot + 4 * sg) % 28, 26);
#pragma unroll
    for (int p = 0; p < 10; ++p) glds16(wdb + (size_t)se * (64 * D_ * 2) + boffs[p], ring + slot * K2_BS + p * 1024);
    const char* hs = hb + (size_t)se * 2048;
#pragma unroll
    for (int i = 0; i < 4; ++i) af[slot][0][i] = gld128a<0>(hs, aoffy[i]);
#pragma unroll
    for (int i = 0; i < 4; ++i) af[slot][1][i] = gld128a<1024>(hs, aoffy[i]);
    __builtin_amdgcn_sched_barrier(0);
  };
  const char* wpb = (const char*)wp + (size_t)(160 * wid) * D_ * 2;
  auto loadS = [&](auto SG) {
    constexpr int sg = SG.value, slot = (sg + 1) % 3;
#pragma unroll
    for (int p = 0; p < (sg == 2 ? 5 : 10); ++p)
      __builtin_amdgcn_global_load_lds((const unsigned*)(wpb + (size_t)sg * (64 * D_ * 2) + boffs[p]), (unsigned*)(ring + slot * K2_BS + p * 1024), 16, 0, 16);
    __builtin_amdgcn_sched_barrier(0);
  };

  const unsigned lbase = lds_addr(smem);
  const int bq = l15 >> 2, bp = lane & 3;
  const unsigned boffc = lbase + wid * K2_WREG + (8 * g + bq) * 160 + bp * 8;
  const unsigned boff0 = boffc + 640 * (g & 1), boff1 = boffc + 640 * ((g & 1) ^ 1);

  f32x4 accy[4][5], accs[4][5];
#pragma unroll
  for (int i = 0; i < 4; ++i)
#pragma unroll
    for (int j = 0; j < 5; ++j) { accy[i][j] = (f32x4){0.f, 0.f, 0.f, 0.f}; accs[i][j] = (f32x4){0.f, 0.f, 0.f, 0.f}; }

  s16x4 wl[2][5], wh[2][5];
  auto rdB = [&](auto SL, auto S) {
    constexpr int slot = SL.value, s = S.value;
    sfor<5>([&](auto J) {
      constexpr int j = J.value;
      wl[s][j] = tr_rd<slot * K2_BS + s * 5120 + j * 32>(boff0);
      wh[s][j] = tr_rd<slot * K2_BS + s * 5120 + j * 32>(boff1);
    });
  };
  using I0 = std::integral_constant<int, 0>; using I1 = std::integral_constant<int, 1>; using I2 = std::integral_constant<int, 2>;
  using I3 = std::integral_constant<int, 3>; using I4 = std::integral_constant<int, 4>; using I5 = std::integral_constant<int, 5>;
  using I6 = std::integral_constant<int, 6>;
  auto stageY = [&](auto SG) {
    constexpr int slot = SG.value % 3;
    using SL = std::integral_constant<int, slot>;
    if ((rot + 4 * SG.value) % 28 == 27) return;
    rdB(SL{}, I0{});
    rdB(SL{}, I1{});
    WAIT_L(10);
#pragma unroll
    for (int j = 0; j < 5; ++j) {
      bf16x8 wf = cat8(wl[0][j], wh[0][j]);
#pragma unroll
      for (int i = 0; i < 4; ++i) accy[i][j] = __builtin_amdgcn_mfma_f32_16x16x32_bf16(wf, af[slot][0][i], accy[i][j], 0, 0, 0);
    }
    WAIT_L(0);
#pragma unroll
    for (int j = 0; j < 5; ++j) {
      bf16x8 wf = cat8(wl[1][j], wh[1][j]);
#pragma unroll
      for (int i = 0; i < 4; ++i) accy[i][j] = __builtin_amdgcn_mfma_f32_16x16x32_bf16(wf, af[slot][1][i], accy[i][j], 0, 0, 0);
    }
    __builtin_amdgcn_sched_barrier(0);
  };

  loadY(I0{});
  loadY(I1{});
  float* kvs = (float*)(smem + K2_OFF_KV);
  float* zs = (float*)(smem + K2_OFF_ZS + wid * 1024);
  WAIT_V(36);
  {
    f32x4 a = __builtin_bit_cast(f32x4, kq[0]), z = __builtin_bit_cast(f32x4, zq[0]);
#pragma unroll
    for (int r = 1; r < 8; ++r) { a += __builtin_bit_cast(f32x4, kq[r]); z += __builtin_bit_cast(f32x4, zq[r]); }
#pragma unroll
    for (int e = 0; e < 4; ++e) { a[e] = rbf(a[e]); z[e] = rbf(z[e]); }
    if (tid < 105) *(f32x4*)(kvs + 4 * tid) = a;
    if (tid < 200) {
      const int e = tid / 10, ch = tid - e * 10;
      *(bf16x8*)(smem + K2_OFF_WO + (e ^ (((e >> 3) & 1) << 2)) * 160 + ch * 16) = woq;
    }
    if (tid < 120) *(u32x4*)(smem + K2_OFF_WO + 3200 + tid * 16) = (u32x4){0u, 0u, 0u, 0u};
    *(f32x4*)(zs + 4 * lane) = z;
  }
  asm volatile("s_waitcnt lgkmcnt(0)" ::: "memory");
  BAR();
  {
    const unsigned wob = lbase + K2_OFF_WO + (8 * g + bq) * 160 + bp * 8;
    const unsigned wo0 = wob + 640 * (g & 1), wo1 = wob + 640 * ((g & 1) ^ 1);
    s16x4 a0 = tr_rd<0>(wo0 + wid * 32), a1 = tr_rd<0>(wo1 + wid * 32), b0 = tr_rd<128>(wo0), b1 = tr_rd<128>(wo1);
    bf16x8 yk[2];
#pragma unroll
    for (int t = 0; t < 2; ++t) {
      const int d = 16 * t + l15, dc = min(d, 19);
      f32x4 k0 = *(const f32x4*)(kvs + dc * DH + min(8 * g, 16)), k1 = *(const f32x4*)(kvs + dc * DH + min(8 * g + 4, 16));
      const bool v0 = d < DH && g < 3, v1 = d < DH && g < 2;
      u32x4 u = {v0 ? pack2(k0[0], k0[1]) : 0u, v0 ? pack2(k0[2], k0[3]) : 0u, v1 ? pack2(k1[0], k1[1]) : 0u, v1 ? pack2(k1[2], k1[3]) : 0u};
      yk[t] = __builtin_bit_cast(bf16x8, u);
    }
    WAIT_L(0);
    const bf16x8 wa = cat8(a0, a1), wb = cat8(b0, b1);
#pragma unroll
    for (int t = 0; t < 2; ++t) {
      const f32x4 z4 = {0.f, 0.f, 0.f, 0.f};
      f32x4 da = __builtin_amdgcn_mfma_f32_16x16x32_bf16(wa, yk[t], z4, 0, 0, 0);
      f32x4 db = __builtin_amdgcn_mfma_f32_16x16x32_bf16(wb, yk[t], z4, 0, 0, 0);
      const int d = 16 * t + l15;
      u16* dst = wp + (size_t)(ph * DH + d) * D_ + pj * 80 + 4 * g;
      unsigned long long va = (unsigned long long)pack2(da[0], da[1]) | ((unsigned long long)pack2(da[2], da[3]) << 32);
      unsigned long long vb = (unsigned long long)pack2(db[0], db[1]) | ((unsigned long long)pack2(db[2], db[3]) << 32);
      if (d < DH) __hip_atomic_store((unsigned long long*)(dst + 16 * wid), va, __ATOMIC_RELAXED, __HIP_MEMORY_SCOPE_AGENT);
      if (d < DH && g == wid) __hip_atomic_store((unsigned long long*)(dst + 64), vb, __ATOMIC_RELAXED, __HIP_MEMORY_SCOPE_AGENT);
    }
  }
  __builtin_amdgcn_sched_barrier(0);
  bf16x8 pf[5][4];
  {
    const char* pqb = (const char*)pq + (size_t)(5 * wid) * 1024;
    sfor<5>([&](auto U) {
#pragma unroll
      for (int i = 0; i < 4; ++i) pf[U.value][i] = gld128<0>(pqb + U.value * 1024, poff[i]);
    });
  }
  loadY(I2{});
  WAIT_V(60);
  stageY(I0{});
  loadY(I3{});
  WAIT_V(56);
  if (lane == 0) {
    unsigned old;
    asm volatile("ds_add_rtn_u32 %0, %1, %2\n\ts_waitcnt lgkmcnt(0)" : "=&v"(old) : "v"(lbase + K2_OFF_CNT), "v"(1u) : "memory");
    if (old == 3) __hip_atomic_store(flags + pj * 32 + ph, 1u, __ATOMIC_RELAXED, __HIP_MEMORY_SCOPE_AGENT);
  }
  stageY(I1{});
  loadY(I4{});
  WAIT_V(36);
  stageY(I2{});
  unsigned fl = gld32_sc1(flags + cb * 32 + 8 * wid, (unsigned)(lane & 7) * 4u);
  loadY(I5{});
  float* rden = (float*)(smem + K2_OFF_RD) + wid * 512;
  {
    f32x4 dn[4];
#pragma unroll
    for (int i = 0; i < 4; ++i) dn[i] = (f32x4){0.f, 0.f, 0.f, 0.f};
#pragma unroll
    for (int u = 0; u < 5; ++u) {
      f32x4 z0 = *(const f32x4*)(zs + 32 * u + 8 * g), z1 = *(const f32x4*)(zs + 32 * u + 8 * g + 4);
      float zv[8] = {z0[0], z0[1], z0[2], z0[3], z1[0], z1[1], z1[2], z1[3]};
      const int k0 = 32 * u + 8 * g;
      const int h0 = k0 / DH, lim = (h0 + 1) * DH - k0;
      const bool m0_ = (h0 == l15), m1_ = (h0 + 1 == l15);
      u32x4 zm;
#pragma unroll
      for (int e = 0; e < 4; ++e) {
        bool m = (2 * e < lim) ? m0_ : m1_;
        zm[e] = m ? pack2(zv[2 * e], zv[2 * e + 1]) : 0u;
      }
      bf16x8 zz = __builtin_bit_cast(bf16x8, zm);
#pragma unroll
      for (int i = 0; i < 4; ++i) dn[i] = __builtin_amdgcn_mfma_f32_16x16x32_bf16(zz, pf[u][i], dn[i], 0, 0, 0);
    }
    if (g < 2) {
#pragma unroll
      for (int i = 0; i < 4; ++i) {
        f32x4 r;
#pragma unroll
        for (int e = 0; e < 4; ++e) r[e] = __builtin_amdgcn_rcpf(rbf(rbf(dn[i][e]) + 1e-6f));
        *(f32x4*)(rden + (i * 16 + l15) * 8 + 4 * g) = r;
      }
    }
    __builtin_amdgcn_s_waitcnt(0xc07f);
    __builtin_amdgcn_wave_barrier();
#pragma unroll
    for (int u = 0; u < 5; ++u) {
      const int k0 = 32 * u + 8 * g;
      const int h0 = k0 / DH;
      const int lim = (h0 + 1) * DH - k0;
#pragma unroll
      for (int i = 0; i < 4; ++i) {
        const float* rp = rden + (i * 16 + l15) * 8 + h0;
        float r0 = rp[0], r1 = rp[1];
        u32x4 uu = __builtin_bit_cast(u32x4, pf[u][i]);
        u32x4 o;
#pragma unroll
        for (int e = 0; e < 4; ++e) {
          float r = (2 * e < lim) ? r0 : r1;
          o[e] = pack2(bflo(uu[e]) * r, bfhi(uu[e]) * r);
        }
        pf[u][i] = __builtin_bit_cast(bf16x8, o);
      }
    }
    __builtin_amdgcn_s_waitcnt(0xc07f);
    __builtin_amdgcn_sched_barrier(0);
  }
  WAIT_V(37);
  stageY(I3{});
  loadY(I6{});
  WAIT_V(36);
  if (!__all(fl == 1u)) {
    for (;;) {
      unsigned f2 = gld32_sc1(flags + cb * 32 + 8 * wid, (unsigned)(lane & 7) * 4u);
      WAIT_V(0);
      if (__all(f2 == 1u)) break;
      __builtin_amdgcn_s_sleep(2);
    }
  }
  stageY(I4{});
  loadS(I0{});
  WAIT_V(28);
  stageY(I5{});
  loadS(I1{});
  WAIT_V(20);
  stageY(I6{});
  loadS(I2{});
  u32x2 xv[5];
#pragma unroll
  for (int j = 0; j < 5; ++j) xv[j] = *(const u32x2*)(x + (size_t)orow * D_ + n0 + j * 16 + 4 * g);
  __builtin_amdgcn_sched_barrier(0);
  {
    WAIT_V(20);
    rdB(I1{}, I0{});
    rdB(I1{}, I1{});
    WAIT_L(10);
#pragma unroll
    for (int j = 0; j < 5; ++j) {
      bf16x8 wf = cat8(wl[0][j], wh[0][j]);
#pragma unroll
      for (int i = 0; i < 4; ++i) accs[i][j] = __builtin_amdgcn_mfma_f32_16x16x32_bf16(wf, pf[0][i], accs[i][j], 0, 0, 0);
    }
    WAIT_L(0);
#pragma unroll
    for (int j = 0; j < 5; ++j) {
      bf16x8 wf = cat8(wl[1][j], wh[1][j]);
#pragma unroll
      for (int i = 0; i < 4; ++i) accs[i][j] = __builtin_amdgcn_mfma_f32_16x16x32_bf16(wf, pf[1][i], accs[i][j], 0, 0, 0);
    }
    __builtin_amdgcn_sched_barrier(0);
    WAIT_V(10);
    rdB(I2{}, I0{});
    rdB(I2{}, I1{});
    WAIT_L(10);
#pragma unroll
    for (int j = 0; j < 5; ++j) {
      bf16x8 wf = cat8(wl[0][j], wh[0][j]);
#pragma unroll
      for (int i = 0; i < 4; ++i) accs[i][j] = __builtin_amdgcn_mfma_f32_16x16x32_bf16(wf, pf[2][i], accs[i][j], 0, 0, 0);
    }
    WAIT_L(0);
#pragma unroll
    for (int j = 0; j < 5; ++j) {
      bf16x8 wf = cat8(wl[1][j], wh[1][j]);
#pragma unroll
      for (int i = 0; i < 4; ++i) accs[i][j] = __builtin_amdgcn_mfma_f32_16x16x32_bf16(wf, pf[3][i], accs[i][j], 0, 0, 0);
    }
    __builtin_amdgcn_sched_barrier(0);
    WAIT_V(5);
    rdB(I0{}, I0{});
    WAIT_L(0);
#pragma unroll
    for (int j = 0; j < 5; ++j) {
      bf16x8 wf = cat8(wl[0][j], wh[0][j]);
#pragma unroll
      for (int i = 0; i < 4; ++i) accs[i][j] = __builtin_amdgcn_mfma_f32_16x16x32_bf16(wf, pf[4][i], accs[i][j], 0, 0, 0);
    }
  }
#pragma unroll
  for (int i = 1; i < 4; ++i)
#pragma unroll
    for (int j = 0; j < 5; ++j) {
      *(f32x4*)(ring + (i - 1) * K2_BS + j * 1024 + lane * 16) = accy[i][j];
      *(f32x4*)(ring + (i - 1) * K2_BS + (5 + j) * 1024 + lane * 16) = accs[i][j];
    }
  __syncthreads();
#pragma unroll
  for (int i = 1; i < 4; ++i) {
    const char* src = smem + ((wid - i) & 3) * K2_WREG + (i - 1) * K2_BS + lane * 16;
#pragma unroll
    for (int j = 0; j < 5; ++j) {
      f32x4 a = *(const f32x4*)(src + j * 1024);
      f32x4 b = *(const f32x4*)(src + (5 + j) * 1024);
      accy[0][j] += a;
      accs[0][j] += b;
    }
  }
#pragma unroll
  for (int j = 0; j < 5; ++j) {
    size_t o = (size_t)orow * D_ + n0 + j * 16 + 4 * g;
    float xs[4] = {bflo(xv[j][0]), bfhi(xv[j][0]), bflo(xv[j][1]), bfhi(xv[j][1])};
    float r[4];
#pragma unroll
    for (int e = 0; e < 4; ++e) {
      float y = rbf(accy[0][j][e]);
      float gt = rbf(y * __builtin_amdgcn_rcpf(1.0f + __expf(-y)));
      float sv = rbf(accs[0][j][e]);
      r[e] = xs[e] + rbf(sv * gt);
    }
    u32x2 ov = {pack2(r[0], r[1]), pack2(r[2], r[3])};
    *(u32x2*)(out + o) = ov;
  }
}

extern "C" void kernel_launch(void* const* d_in, const int* in_sizes, int n_in, void* d_out, int out_size,
                              void* d_ws, size_t ws_size, hipStream_t stream) {
  const u16* x = (const u16*)d_in[0];
  const u16* wqkv = (const u16*)d_in[1];
  const u16* wo = (const u16*)d_in[2];
  const u16* wg = (const u16*)d_in[4];
  const u16* wu = (const u16*)d_in[5];
  const u16* wd = (const u16*)d_in[6];
  char* ws = (char*)d_ws;
  u16* pq = (u16*)(ws);
  u16* h = (u16*)(ws + (4 << 20));
  float* kvp = (float*)(ws + (12 << 20));
  u16* wp = (u16*)(ws + (13 << 20));
  unsigned* flags = (unsigned*)(ws + (14 << 20));
  hipFuncSetAttribute((const void*)k1_kernel, hipFuncAttributeMaxDynamicSharedMemorySize, K1_LDS);
  hipFuncSetAttribute((const void*)k2_kernel, hipFuncAttributeMaxDynamicSharedMemorySize, K2_LDS);
  k1_kernel<<<240, 512, K1_LDS, stream>>>(x, wqkv, wg, wu, pq, h, kvp, flags, wd, wo);
  k2_kernel<<<256, 256, K2_LDS, stream>>>(x, h, wd, pq, kvp, wo, wp, flags, (u16*)d_out);
}
```

```cpp
#include <hip/hip_runtime.h>
#include <hip/hip_bf16.h>
#include <stdint.h>
#include <utility>

typedef unsigned short u16;
typedef __attribute__((ext_vector_type(8))) short bf16x8;
typedef __attribute__((ext_vector_type(4))) short s16x4;
typedef __attribute__((ext_vector_type(4))) float f32x4;
typedef __attribute__((ext_vector_type(2))) __bf16 bf2;
typedef __attribute__((ext_vector_type(4))) unsigned u32x4;
typedef __attribute__((ext_vector_type(2))) unsigned u32x2;
#define LAS __attribute__((address_space(3)))

constexpr int S_ = 2048, D_ = 640, F_ = 1728, NH = 32, DH = 20;
constexpr int KVSZ = DH * DH + DH;

__device__ __forceinline__ unsigned pack2(float a, float b) {
  bf2 v = {(__bf16)a, (__bf16)b};
  return __builtin_bit_cast(unsigned, v);
}
__device__ __forceinline__ float rbf(float a) { return (float)(__bf16)a; }
__device__ __forceinline__ float bflo(unsigned u) { return __uint_as_float(u << 16); }
__device__ __forceinline__ float bfhi(unsigned u) { return __uint_as_float(u & 0xffff0000u); }
__device__ __forceinline__ float dot_ones(unsigned a, float c) {
  bf2 one = {(__bf16)1.0f, (__bf16)1.0f};
  return __builtin_amdgcn_fdot2_f32_bf16(__builtin_bit_cast(bf2, a), one, c, false);
}
__device__ __forceinline__ float dot_self(unsigned a, float c) {
  bf2 v = __builtin_bit_cast(bf2, a);
  return __builtin_amdgcn_fdot2_f32_bf16(v, v, c, false);
}
__device__ __forceinline__ void glds16(const void* g, void* l) {
  __builtin_amdgcn_global_load_lds((const unsigned*)g, (unsigned*)l, 16, 0, 0);
}
template <class F, int... I> __device__ __forceinline__ void sfor_impl(F&& f, std::integer_sequence<int, I...>) {
  (f(std::integral_constant<int, I>{}), ...);
}
template <int N, class F> __device__ __forceinline__ void sfor(F&& f) { sfor_impl(f, std::make_integer_sequence<int, N>{}); }
#define WAIT_V(n) asm volatile("s_waitcnt vmcnt(" #n ")" ::: "memory")
#define WAIT_VS(n) do { WAIT_V(n); __builtin_amdgcn_sched_barrier(0); } while (0)
#define BAR() __builtin_amdgcn_s_barrier()
template <int OFF> __device__ __forceinline__ s16x4 tr_rd(unsigned a) {
  s16x4 r; asm volatile("ds_read_b64_tr_b16 %0, %1 offset:%2" : "=&v"(r) : "v"(a), "i"(OFF) : "memory"); return r;
}
template <int OFF> __device__ __forceinline__ bf16x8 lds_rd128(unsigned a) {
  bf16x8 r; asm volatile("ds_read_b128 %0, %1 offset:%2" : "=&v"(r) : "v"(a), "i"(OFF) : "memory"); return r;
}
template <int OFF> __device__ __forceinline__ bf16x8 gld128(const void* sbase, unsigned voff) {
  bf16x8 r; asm volatile("global_load_dwordx4 %0, %1, %2 offset:%3" : "=&v"(r) : "v"(voff), "s"(sbase), "i"(OFF) : "memory"); return r;
}
template <int OFF> __device__ __forceinline__ bf16x8 gld128a(const void* sbase, unsigned voff) {
  bf16x8 r; asm volatile("global_load_dwordx4 %0, %1, %2 offset:%3" : "=&a"(r) : "v"(voff), "s"(sbase), "i"(OFF) : "memory"); return r;
}
__device__ __forceinline__ bf16x8 cat8(s16x4 a, s16x4 b) { return __builtin_shufflevector(a, b, 0, 1, 2, 3, 4, 5, 6, 7); }
#define WAIT_L(n) do { asm volatile("s_waitcnt lgkmcnt(" #n ")" ::: "memory"); __builtin_amdgcn_sched_barrier(0); } while (0)
__device__ __forceinline__ unsigned lds_addr(const void* p) { return (unsigned)(size_t)(LAS const char*)p; }
__device__ __forceinline__ float phi_bf(float t) {
  float p = t > 0.f ? rbf(t + 1.0f) : rbf(__expf(t));
  return rbf(p + 1e-6f);
}

constexpr int K1_ABYTES = 256 * 64 * 2;
constexpr int K1_BBYTES = 64 * 192 * 2;
constexpr int K1_OFF_B = 3 * K1_ABYTES;
constexpr int K1_OFF_MISC = K1_OFF_B + 2 * K1_BBYTES;
constexpr int K1_OFF_DUMMY = K1_OFF_MISC;
constexpr int K1_OFF_MEAN = K1_OFF_DUMMY + 1024;
constexpr int K1_OFF_CSF = K1_OFF_MEAN + 2048;
constexpr int K1_LDS = K1_OFF_CSF + 768;
constexpr int K1_OFF_RS = K1_ABYTES;
constexpr int K1_OFF_CS = 2 * K1_ABYTES;

template <int ROLE>
__device__ __forceinline__ void k1_body(const u16* __restrict__ x, const u16* __restrict__ w0,
                                        const u16* __restrict__ w1, u16* __restrict__ outp,
                                        float* __restrict__ kvp, int rb, int cgl,
                                        const char* __restrict__ wop, char* smem) {
  constexpr int BN = ROLE == 2 ? 192 : 160;
  constexpr int NB = BN / 16;
  constexpr int WM = ROLE == 1 ? 8 : 4;
  constexpr int WN = 8 / WM;
  constexpr int MT = 16 / WM;
  constexpr int NT = NB / WN;
  constexpr int BPIECES = 64 * BN * 2 / 1024;
  constexpr int LDW = ROLE == 2 ? F_ : 3 * D_;
  constexpr int RBI = ROLE == 2 ? 192 : ROLE == 0 ? 320 : 160;
  constexpr int CPR = RBI / 16;
  constexpr int PPI = 64 * RBI / 1024;
  constexpr int IMGB = 64 * RBI;

  const int tid = threadIdx.x;
  const int wid = __builtin_amdgcn_readfirstlane(tid >> 6);
  const int lane = tid & 63;
  const int g = lane >> 4, l15 = lane & 15;
  const int wm = wid % WM, wn = wid / WM;
  const int m0 = rb * 256;
  const int CSOFF = 2 * wn;

  const char* abase = (const char*)(x + (size_t)m0 * D_);
  const unsigned aoffg = (unsigned)(((wid * 8 + (lane >> 3)) * D_ + (((lane & 7) ^ (lane >> 3)) * 8)) * 2);
  const char* bptr[3];
#pragma unroll
  for (int i = 0; i < 3; ++i) {
    int p = wid + 8 * i;
    if (p >= BPIECES) p = wid;
    const int img = p / PPI, c = (p - img * PPI) * 64 + lane;
    const int rho = c / CPR, chp = c - rho * CPR;
    const int k = ROLE == 1 ? (rho ^ (((rho >> 3) & 1) << 2)) : rho;
    const int ch = ROLE == 1 ? chp : (chp ^ (2 * ((rho >> 3) & 1)));
    const u16* wp;
    if (ROLE == 0) wp = w0 + cgl * 160;
    else if (ROLE == 1) wp = w0 + (img ? 2 * D_ : D_) + cgl * 80;
    else wp = (img ? w1 : w0) + cgl * 96;
    bptr[i] = (const char*)(wp + (size_t)k * LDW + ch * 8);
  }
  auto loadA = [&](int t, int slot, int i) {
    glds16(abase + (size_t)(i * 64 * D_ * 2 + t * 128) + aoffg, smem + slot * K1_ABYTES + (wid + 8 * i) * 1024);
  };
  auto loadB = [&](int t, int slot, int i) {
    if (wid + 8 * i < BPIECES) {
      glds16(bptr[i] + (size_t)t * (64 * LDW * 2), smem + K1_OFF_B + slot * K1_BBYTES + (wid + 8 * i) * 1024);
    }
  };
  auto stageA = [&](int t, int slot) {
#pragma unroll
    for (int i = 0; i < 4; ++i) loadA(t, slot, i);
  };
  auto stageB = [&](int t, int slot) {
#pragma unroll
    for (int i = 0; i < 3; ++i) loadB(t, slot, i);
  };

  const char* pfo = wop + (size_t)((blockIdx.x * 8 + wid) % 800) * 1024 + lane * 16;
  stageA(0, 0);
  stageB(0, 0);
  stageA(1, 1);

  const int arow = wm * (MT * 16) + l15;
  const unsigned lbase = lds_addr(smem);
  const unsigned aoff0 = lbase + arow * 128 + ((g ^ (lane & 7)) * 16);
  const int bq = l15 >> 2, bp = lane & 3;
  constexpr int NBJ = ROLE == 2 ? 3 : ROLE == 0 ? 5 : 2;
  unsigned bj[NBJ];
  if (ROLE == 1) {
    bj[0] = lbase + K1_OFF_B + (8 * g + bq + 4 * (g & 1)) * RBI + bp * 8;
    bj[1] = lbase + K1_OFF_B + (8 * g + bq + 4 * ((g & 1) ^ 1)) * RBI + bp * 8;
  } else {
#pragma unroll
    for (int j = 0; j < NBJ; ++j) bj[j] = lbase + K1_OFF_B + (8 * g + bq) * RBI + (((wn * NBJ + j) ^ (g & 1)) * 32) + bp * 8;
  }

  f32x4 acc[MT][NT];
#pragma unroll
  for (int i = 0; i < MT; ++i)
#pragma unroll
    for (int j = 0; j < NT; ++j) acc[i][j] = (f32x4){0.f, 0.f, 0.f, 0.f};
  float rs[MT], rq[MT], cs[NT];
#pragma unroll
  for (int i = 0; i < MT; ++i) { rs[i] = 0.f; rq[i] = 0.f; }
#pragma unroll
  for (int j = 0; j < NT; ++j) cs[j] = 0.f;

  bf16x8 xf[2][MT];
  s16x4 wl[2][NT], wh[2][NT];
  auto rd = [&](auto S, unsigned soa, unsigned sob) {
    constexpr int s = S.value;
    const unsigned aa = (aoff0 ^ (s * 64)) + soa;
    sfor<MT>([&](auto I) { xf[s][I.value] = lds_rd128<I.value * 2048>((I.value & 1) ? aa - wn * 2048 : aa + wn * 2048); });
    unsigned bs[NBJ];
#pragma unroll
    for (int j = 0; j < NBJ; ++j) bs[j] = bj[j] + sob;
    sfor<NT>([&](auto J) {
      constexpr int j = J.value;
      if constexpr (ROLE == 1) {
        constexpr int off = (j / 5) * IMGB + s * 32 * RBI + (j % 5) * 32;
        wl[s][j] = tr_rd<off>(bs[0]);
        wh[s][j] = tr_rd<off>(bs[1]);
      } else {
        constexpr int off = (j / NBJ) * IMGB + s * 32 * RBI;
        wl[s][j] = tr_rd<off>(bs[j % NBJ]);
        wh[s][j] = tr_rd<off + 4 * RBI>(bs[j % NBJ]);
      }
    });
  };
  auto rd1 = [&](auto S, auto R, unsigned aa, const unsigned* bs) {
    constexpr int s = S.value, r = R.value;
    if constexpr (r < MT) {
      xf[s][r] = lds_rd128<r * 2048>((r & 1) ? aa - wn * 2048 : aa + wn * 2048);
    } else {
      constexpr int j = (r - MT) / 2, hi = (r - MT) & 1;
      if constexpr (ROLE == 1) {
        constexpr int off = (j / 5) * IMGB + s * 32 * RBI + (j % 5) * 32;
        if constexpr (hi) wh[s][j] = tr_rd<off>(bs[1]); else wl[s][j] = tr_rd<off>(bs[0]);
      } else {
        constexpr int off = (j / NBJ) * IMGB + s * 32 * RBI;
        if constexpr (hi) wh[s][j] = tr_rd<off + 4 * RBI>(bs[j % NBJ]); else wl[s][j] = tr_rd<off>(bs[j % NBJ]);
      }
    }
  };
  auto mm = [&](auto S, auto LDX, int t, int nslot, bool do_cs, auto RSX, unsigned soa, unsigned sob) {
    constexpr int s = S.value, LD = LDX.value, RS = RSX.value;
    constexpr int NRDX = MT + 2 * NT;
    const unsigned aa = (aoff0 ^ ((RS & 1) * 64)) + soa;
    unsigned bs[NBJ];
#pragma unroll
    for (int j = 0; j < NBJ; ++j) bs[j] = bj[j] + sob;
    bf16x8 wf[NT];
#pragma unroll
    for (int j = 0; j < NT; ++j) wf[j] = cat8(wl[s][j], wh[s][j]);
    constexpr int NM = MT * NT;
    constexpr int ND = (WN == 2) ? (MT / 2) * 8 : MT * 8;
    sfor<NM>([&](auto X) {
      constexpr int xi = X.value, i = xi / NT, j = xi % NT;
      if (ROLE == 1) acc[i][j] = __builtin_amdgcn_mfma_f32_16x16x32_bf16(xf[s][i], wf[j], acc[i][j], 0, 0, 0);
      else acc[i][j] = __builtin_amdgcn_mfma_f32_16x16x32_bf16(wf[j], xf[s][i], acc[i][j], 0, 0, 0);
      constexpr int d0 = xi * ND / NM, d1 = (xi + 1) * ND / NM;
      sfor<d1 - d0>([&](auto Y) {
        constexpr int d = d0 + Y.value;
        constexpr int NTL = ND / 8;
        constexpr int q = (d >> 1) % NTL, e = (d >> 1) / NTL;
        constexpr int ii = (WN == 2) ? 2 * q : q;
        unsigned u = __builtin_bit_cast(u32x4, xf[s][ii])[e];
        if (d & 1) rq[q] = dot_self(u, rq[q]); else rs[q] = dot_ones(u, rs[q]);
      });
      if constexpr (RS < 2) {
        constexpr int NMR = (NM * 3) / 4;
        constexpr int r0 = xi < NMR ? xi * NRDX / NMR : NRDX, r1 = xi + 1 < NMR ? (xi + 1) * NRDX / NMR : NRDX;
        sfor<r1 - r0>([&](auto Y) { rd1(std::integral_constant<int, RS & 1>{}, std::integral_constant<int, r0 + Y.value>{}, aa, bs); });
      }
      constexpr int P = NM / 4;
      if constexpr (LD == 0 && (xi == 1 || xi == 1 + P || xi == 1 + 2 * P)) { if (t + 1 < 10) loadB(t + 1, (t + 1) & 1, (xi - 1) / P); else if (xi == 1) glds16(pfo, smem + K1_OFF_DUMMY); }
      if constexpr (LD == 0 && xi == 1 + 3 * P) { if (t + 2 < 10) loadA(t + 2, nslot, 0); }
      if constexpr (LD == 1 && (xi == 1 || xi == 1 + P || xi == 1 + 2 * P)) {
        if (t + 2 < 10) loadA(t + 2, nslot, 1 + (xi - 1) / P);
      }
      __builtin_amdgcn_sched_barrier(0);
    });
    if (do_cs) {
#pragma unroll
      for (int j = 0; j < NT; ++j) {
        u32x4 u = __builtin_bit_cast(u32x4, wf[j]);
#pragma unroll
        for (int e = 0; e < 4; ++e) cs[j] = dot_ones(u[e], cs[j]);
      }
    }
  };
  using I0 = std::integral_constant<int, 0>; using I1 = std::integral_constant<int, 1>; using I2 = std::integral_constant<int, 2>;
  constexpr int NRD = MT + 2 * NT;

  int aslot = 0;
  {
    bool prev_cs = (CSOFF % WM) == wm;
    {
      WAIT_V(4);
      BAR();
      rd(I0{}, 0u, 0u);
      stageB(1, 1);
      loadA(2, 2, 0);
      WAIT_L(0);
      mm(I0{}, I1{}, 0, 2, prev_cs, I1{}, 0u, 0u);
      WAIT_L(0);
      aslot = 1;
    }
#pragma nounroll
    for (int t = 1; t < 10; ++t) {
      if (t < 9) WAIT_V(4); else WAIT_V(0);
      BAR();
      const int nslot = aslot == 0 ? 2 : aslot - 1;
      const unsigned soa = aslot * K1_ABYTES, sob = (t & 1) * K1_BBYTES;
      aslot = aslot == 2 ? 0 : aslot + 1;
      const bool do_cs = ((t + CSOFF) % WM) == wm;
      mm(I1{}, I0{}, t, nslot, prev_cs, I0{}, soa, sob);
      WAIT_L(0);
      mm(I0{}, I1{}, t, nslot, do_cs, I1{}, soa, sob);
      WAIT_L(0);
      prev_cs = do_cs;
    }
    mm(I1{}, I2{}, 10, 0, prev_cs, I2{}, 0u, 0u);
  }

  float* rsum = (float*)(smem + K1_OFF_RS);
  float* csum = (float*)(smem + K1_OFF_CS);
  float* smean = (float*)(smem + K1_OFF_MEAN);
  float* srsd = smean + 256;
  float* scs = (float*)(smem + K1_OFF_CSF);
#pragma unroll
  for (int q = 0; q < (WN == 2 ? MT / 2 : MT); ++q) {
    int r = wm * (MT * 16) + ((WN == 2) ? ((2 * q) ^ wn) : q) * 16 + l15;
    float2 v = {rs[q], rq[q]};
    *(float2*)(rsum + (g * 256 + r) * 2) = v;
  }
#pragma unroll
  for (int j = 0; j < NT; ++j) {
    int nb = (ROLE == 2) ? ((j < 3) ? (wn * 3 + j) : (6 + wn * 3 + (j - 3))) : (wn * NT + j);
    csum[(wm * 4 + g) * 192 + nb * 16 + l15] = cs[j];
  }
  WAIT_L(0);
  BAR();
  if (tid < 256) {
    float a = 0.f, b = 0.f;
#pragma unroll
    for (int w = 0; w < 4; ++w) { float2 v = *(const float2*)(rsum + (w * 256 + tid) * 2); a += v.x; b += v.y; }
    float mean = a * (1.0f / D_);
    float var = fmaxf(b * (1.0f / D_) - mean * mean, 0.f);
    float sd = rbf(sqrtf(rbf(rbf(var) + 1e-5f)));
    smean[tid] = rbf(mean);
    srsd[tid] = 1.0f / sd;
  } else if (tid - 256 < BN) {
    int n = tid - 256;
    float a = 0.f;
#pragma unroll
    for (int w = 0; w < WM * 4; ++w) a += csum[w * 192 + n];
    scs[n] = a;
  }
  WAIT_L(0);
  BAR();

  if (ROLE == 0) {
    constexpr int PITCH = 336;
#pragma unroll
    for (int i = 0; i < MT; ++i) {
      int r = wm * 64 + (i ^ wn) * 16 + l15;
      float mean = smean[r], rsd = srsd[r];
#pragma unroll
      for (int j = 0; j < NT; ++j) {
        int n = (wn * NT + j) * 16 + 4 * g;
        f32x4 c4 = *(const f32x4*)(scs + n);
        float p[4];
#pragma unroll
        for (int e = 0; e < 4; ++e) p[e] = phi_bf(rbf((acc[i][j][e] - mean * c4[e]) * rsd));
        u32x2 o = {pack2(p[0], p[1]), pack2(p[2], p[3])};
        *(u32x2*)(smem + r * PITCH + n * 2) = o;
      }
    }
    __syncthreads();
#pragma unroll
    for (int it = 0; it < 10; ++it) {
      int o = tid + 512 * it, blk = o >> 6, ln = o & 63;
      int rt = blk / 5, un = blk - rt * 5;
      u32x4 v = *(const u32x4*)(smem + (rt * 16 + (ln & 15)) * PITCH + (un * 4 + (ln >> 4)) * 16);
      *(u32x4*)(outp + ((size_t)((rb * 16 + rt) * 20 + cgl * 5 + un) * 64 + ln) * 8) = v;
    }
  } else if (ROLE == 2) {
    constexpr int PITCH = 208;
#pragma unroll
    for (int i = 0; i < MT; ++i) {
      int r = wm * 64 + (i ^ wn) * 16 + l15;
      float mean = smean[r], rsd = srsd[r];
#pragma unroll
      for (int j = 0; j < 3; ++j) {
        int n = (wn * 3 + j) * 16 + 4 * g;
        f32x4 cg4 = *(const f32x4*)(scs + n);
        f32x4 cu4 = *(const f32x4*)(scs + 96 + n);
        float hv[4];
#pragma unroll
        for (int e = 0; e < 4; ++e) {
          float gv = rbf((acc[i][j][e] - mean * cg4[e]) * rsd);
          float uv = rbf((acc[i][j + 3][e] - mean * cu4[e]) * rsd);
          float sg = rbf(__builtin_amdgcn_rcpf(1.0f + __expf(-gv)));
          hv[e] = sg * uv;
        }
        u32x2 o = {pack2(hv[0], hv[1]), pack2(hv[2], hv[3])};
        *(u32x2*)(smem + r * PITCH + n * 2) = o;
      }
    }
    __syncthreads();
#pragma unroll
    for (int it = 0; it < 6; ++it) {
      int o = tid + 512 * it, blk = o >> 6, ln = o & 63;
      int rt = blk / 3, un = blk - rt * 3;
      u32x4 v = *(const u32x4*)(smem + (rt * 16 + (ln & 15)) * PITCH + (un * 4 + (ln >> 4)) * 16);
      *(u32x4*)(outp + ((size_t)((rb * 16 + rt) * 54 + cgl * 3 + un) * 64 + ln) * 8) = v;
    }
  } else {
    bf16x8 fr[10];
    float mean[2][4], rsd[2][4];
#pragma unroll
    for (int i = 0; i < 2; ++i)
#pragma unroll
      for (int e = 0; e < 4; ++e) {
        int r = wid * 32 + i * 16 + 4 * g + e;
        mean[i][e] = smean[r];
        rsd[i][e] = srsd[r];
      }
#pragma unroll
    for (int j = 0; j < 10; ++j) {
      float c = scs[j * 16 + l15];
      float v[8];
#pragma unroll
      for (int i = 0; i < 2; ++i)
#pragma unroll
        for (int e = 0; e < 4; ++e) {
          float t = rbf((acc[i][j][e] - mean[i][e] * c) * rsd[i][e]);
          v[i * 4 + e] = (j < 5) ? phi_bf(t) : t;
        }
      u32x4 u = {pack2(v[0], v[1]), pack2(v[2], v[3]), pack2(v[4], v[5]), pack2(v[6], v[7])};
      fr[j] = __builtin_bit_cast(bf16x8, u);
    }
    float* red = (float*)smem;
    float* myred = red + wid * (4 * KVSZ);
    const bf16x8 ones = {0x3F80, 0x3F80, 0x3F80, 0x3F80, 0x3F80, 0x3F80, 0x3F80, 0x3F80};
#pragma unroll
    for (int i = 0; i < 5; ++i) {
#pragma unroll
      for (int j = 0; j < 5; ++j) {
        if (j < i - 1 || j > i + 1) continue;
        f32x4 z4 = {0.f, 0.f, 0.f, 0.f};
        f32x4 d = __builtin_amdgcn_mfma_f32_16x16x32_bf16(fr[i], fr[5 + j], z4, 0, 0, 0);
        int vc = 16 * j + l15, hv = vc / DH, ev = vc - hv * DH;
#pragma unroll
        for (int e = 0; e < 4; ++e) {
          int kc = 16 * i + 4 * g + e, hk = kc / DH, dk = kc - hk * DH;
          if (hk == hv) myred[hk * KVSZ + dk * DH + ev] = d[e];
        }
      }
      f32x4 z4 = {0.f, 0.f, 0.f, 0.f};
      f32x4 d = __builtin_amdgcn_mfma_f32_16x16x32_bf16(fr[i], ones, z4, 0, 0, 0);
      if (l15 == 0) {
#pragma unroll
        for (int e = 0; e < 4; ++e) {
          int kc = 16 * i + 4 * g + e, hk = kc / DH, dk = kc - hk * DH;
          myred[hk * KVSZ + DH * DH + dk] = d[e];
        }
      }
    }
    __syncthreads();
    for (int o = tid; o < 4 * KVSZ; o += 512) {
      float a = 0.f;
#pragma unroll
      for (int w = 0; w < 8; ++w) a += red[w * (4 * KVSZ) + o];
      kvp[(size_t)(rb * NH + cgl * 4) * KVSZ + o] = a;
    }
  }
}

__device__ __forceinline__ void k1_retile(const u16* __restrict__ wd, u16* __restrict__ wdt, int r, char* smem) {
  const int tid = threadIdx.x;
  const int wid = __builtin_amdgcn_readfirstlane(tid >> 6);
  const int lane = tid & 63;
  const int g = lane >> 4, l15 = lane & 15;
  char* area = smem + wid * 10240;
  unsigned boffs[5];
#pragma unroll
  for (int p = 0; p < 5; ++p) {
    int c = p * 64 + lane;
    int rho = c / 10, ch = c - rho * 10;
    int row = rho ^ (((rho >> 3) & 1) << 2);
    boffs[p] = (unsigned)(row * D_ + 8 * ch) * 2u;
  }
  const unsigned boffc = lds_addr(smem) + wid * 10240 + (8 * g + (l15 >> 2)) * 160 + (lane & 3) * 8;
  const unsigned boff0 = boffc + 640 * (g & 1), boff1 = boffc + 640 * ((g & 1) ^ 1);
#pragma unroll
  for (int hf = 0; hf < 2; ++hf) {
#pragma unroll
    for (int s = 0; s < 2; ++s) {
      const int idx = r * 8 + wid + 128 * (2 * hf + s);
      if (idx < 432) {
        const int cb = idx / 54, u = idx - cb * 54;
        const char* src = (const char*)(wd + (size_t)u * 32 * D_ + cb * 80);
#pragma unroll
        for (int p = 0; p < 5; ++p) glds16(src + boffs[p], area + s * 5120 + p * 1024);
      }
    }
    WAIT_V(0);
    sfor<2>([&](auto S) {
      constexpr int s = S.value;
      const int idx = r * 8 + wid + 128 * (2 * hf + s);
      if (idx < 432) {
        s16x4 wl[5], wh[5];
        sfor<5>([&](auto J) {
          wl[J.value] = tr_rd<s * 5120 + J.value * 32>(boff0);
          wh[J.value] = tr_rd<s * 5120 + J.value * 32>(boff1);
        });
        WAIT_L(0);
        char* dst = (char*)wdt + (size_t)idx * 5120 + lane * 16;
#pragma unroll
        for (int j = 0; j < 5; ++j) *(bf16x8*)(dst + j * 1024) = cat8(wl[j], wh[j]);
      }
    });
  }
}

__global__ __launch_bounds__(512, 2) void k1_kernel(const u16* __restrict__ x, const u16* __restrict__ wqkv,
                                                    const u16* __restrict__ wg, const u16* __restrict__ wu,
                                                    u16* __restrict__ pq, u16* __restrict__ h,
                                                    float* __restrict__ kvp, unsigned* __restrict__ flags,
                                                    const u16* __restrict__ wd, const u16* __restrict__ wo,
                                                    u16* __restrict__ wdt) {
  extern __shared__ __attribute__((aligned(16))) char smem[];
  const int bid = blockIdx.x;
  if (bid >= 240) { k1_retile(wd, wdt, bid - 240, smem); return; }
  if (bid == 0) flags[threadIdx.x] = 0u;
  const int lin = (bid & 7) * 30 + (bid >> 3);
  const int cg = lin >> 3, rb = lin & 7;
  if (cg < 18) k1_body<2>(x, wg, wu, h, kvp, rb, cg, (const char*)wo, smem);
  else if (cg < 26) k1_body<1>(x, wqkv, wqkv, pq, kvp, rb, cg - 18, (const char*)wo, smem);
  else k1_body<0>(x, wqkv, wqkv, pq, kvp, rb, cg - 26, (const char*)wo, smem);
}

constexpr int K2_REG = 20480;
constexpr int K2_LDS = 8 * K2_REG;
constexpr int K2_P_KV = 0;
constexpr int K2_P_WO = 13440;
constexpr int K2_P_KS = 18560;
constexpr int K2_S_ZL = 0;
constexpr int K2_S_ZS = 5120;
constexpr int K2_S_RD = 6144;

__device__ __forceinline__ unsigned gld32_sc1(const void* sbase, unsigned voff) {
  unsigned r; asm volatile("global_load_dword %0, %1, %2 sc1" : "=&v"(r) : "v"(voff), "s"(sbase) : "memory"); return r;
}
template <int OFF> __device__ __forceinline__ bf16x8 gld128_sc1(const void* sbase, unsigned voff) {
  bf16x8 r; asm volatile("global_load_dwordx4 %0, %1, %2 offset:%3 sc1" : "=&v"(r) : "v"(voff), "s"(sbase), "i"(OFF) : "memory"); return r;
}

__global__ __launch_bounds__(512, 2) void k2_kernel(const u16* __restrict__ x, const u16* __restrict__ h,
                                                    const u16* __restrict__ wdt, const u16* __restrict__ pq,
                                                    const float* __restrict__ kvp, const u16* __restrict__ wo,
                                                    u16* __restrict__ wp, unsigned* __restrict__ flags,
                                                    u16* __restrict__ out) {
  extern __shared__ __attribute__((aligned(16))) char smem[];
  const int bid = blockIdx.x;
  const int lin = (bid & 7) * 32 + (bid >> 3);
  const int rb = lin >> 3, cb = lin & 7;
  const int m0 = rb * 64, n0 = cb * 80;
  const int tid = threadIdx.x;
  const int wid = __builtin_amdgcn_readfirstlane(tid >> 6);
  const int lane = tid & 63;
  const int hi = wid >> 2;
  using I0 = std::integral_constant<int, 0>; using I1 = std::integral_constant<int, 1>; using I2 = std::integral_constant<int, 2>;
  const unsigned lane16 = (unsigned)lane * 16u;
  auto lid = [&]() { unsigned v = lane16; asm volatile("" : "+v"(v)); return (int)(v >> 4); };

  f32x4 acc[4][5];
  bf16x8 af[3][4], bf[3][5];
  const char* hbi[4];
#pragma unroll
  for (int i = 0; i < 4; ++i) hbi[i] = (const char*)h + (size_t)(rb * 4 + ((i + wid) & 3)) * 54 * 1024;
  const char* wtb = (const char*)wdt + (size_t)cb * 54 * 5120 + 2048;
  const int rot = (7 * cb + 14 * (rb & 3)) % 54;
  auto seof = [&](int q) { int s = q + rot; return s >= 54 ? s - 54 : s; };
  auto ldA = [&](int se, auto SL, auto I) { af[SL.value][I.value] = gld128a<0>(hbi[I.value] + (size_t)se * 1024, lane16); };
  auto ldB = [&](int se, auto SL, auto J, auto HI) {
    bf[SL.value][J.value] = gld128<((J.value + 3 * HI.value) % 5 - 2) * 1024>(wtb + (size_t)se * 5120, lane16);
  };
  auto ldU = [&](int se, auto SL, auto HI) {
    sfor<5>([&](auto J) { ldB(se, SL, J, HI); });
    sfor<4>([&](auto I) { ldA(se, SL, I); });
    __builtin_amdgcn_sched_barrier(0);
  };
  auto unit = [&](auto SL, auto HI, auto LD, int sen) {
    sfor<5>([&](auto J) {
      sfor<4>([&](auto I) {
        constexpr int i = I.value, j = J.value;
        acc[i][j] = __builtin_amdgcn_mfma_f32_16x16x32_bf16(bf[SL.value][j], af[SL.value][i], acc[i][j], 0, 0, 0);
        if constexpr (LD.value) {
          if constexpr (j == 4) ldA(sen, SL, I);
          if constexpr (i == 3) ldB(sen, SL, J, HI);
        }
        __builtin_amdgcn_sched_barrier(0);
      });
    });
  };
  auto zacc = [&]() {
#pragma unroll
    for (int i = 0; i < 4; ++i)
#pragma unroll
      for (int j = 0; j < 5; ++j) acc[i][j] = (f32x4){0.f, 0.f, 0.f, 0.f};
  };

  if (wid < 4) {
    const int ph = bid >> 3, pj = bid & 7;
    char* pm = smem + wid * K2_REG;
    if (wid < 2) {
      __builtin_amdgcn_s_setprio(3);
      const char* kb = (const char*)kvp + (size_t)(ph * KVSZ) * 4 + lane16;
#pragma unroll
      for (int r = 0; r < 8; ++r) {
        glds16(kb + (size_t)r * (NH * KVSZ * 4), pm + K2_P_KV + r * 1680);
        if (lane < 36) glds16(kb + (size_t)r * (NH * KVSZ * 4) + 1024, pm + K2_P_KV + r * 1680 + 1024);
      }
#pragma unroll
      for (int p = 0; p < 4; ++p) {
        const int c = lane + 64 * p, rho = c / 10, ch = c - rho * 10, e = rho ^ (((rho >> 3) & 1) << 2);
        if (c < 200) glds16(wo + (size_t)(ph * DH + e) * D_ + pj * 80 + ch * 8, pm + K2_P_WO + p * 1024);
      }
      __builtin_amdgcn_sched_barrier(0);
    } else {
      __builtin_amdgcn_s_setprio(1);
    }
    BAR();
    int q = 12 * wid;
    if (wid < 2) {
      const int sw = wid;
      const int lane = lid();
      const int g = lane >> 4, l15 = lane & 15;
      float* kvs = (float*)(pm + K2_P_KS);
      if (lane < 60) {
#pragma unroll
        for (int p = 0; p < 2; ++p) *(u32x4*)(pm + K2_P_WO + 3200 + (lane + 60 * p) * 16) = (u32x4){0u, 0u, 0u, 0u};
      }
      WAIT_VS(0);
      {
        const int l1 = min(lane, 35);
        f32x4 a0 = *(const f32x4*)(pm + K2_P_KV + lane * 16), a1 = *(const f32x4*)(pm + K2_P_KV + 1024 + l1 * 16);
#pragma unroll
        for (int r = 1; r < 8; ++r) {
          a0 += *(const f32x4*)(pm + K2_P_KV + r * 1680 + lane * 16);
          a1 += *(const f32x4*)(pm + K2_P_KV + r * 1680 + 1024 + l1 * 16);
        }
#pragma unroll
        for (int e = 0; e < 4; ++e) { a0[e] = rbf(a0[e]); a1[e] = rbf(a1[e]); }
        *(f32x4*)(kvs + 4 * lane) = a0;
        if (lane < 36) *(f32x4*)(kvs + 256 + 4 * lane) = a1;
      }
      WAIT_L(0);
      __builtin_amdgcn_wave_barrier();
      const unsigned wob = lds_addr(pm) + K2_P_WO + (8 * g + (l15 >> 2)) * 160 + (lane & 3) * 8 + (sw ? 96 : 0);
      const unsigned wo0 = wob + 640 * (g & 1), wo1 = wob + 640 * ((g & 1) ^ 1);
      s16x4 al[3], ah[3];
      sfor<3>([&](auto C) { al[C.value] = tr_rd<C.value * 32>(wo0); ah[C.value] = tr_rd<C.value * 32>(wo1); });
      bf16x8 yk[2];
#pragma unroll
      for (int t = 0; t < 2; ++t) {
        const int d = 16 * t + l15, dc = min(d, 19);
        f32x4 k0 = *(const f32x4*)(kvs + dc * DH + min(8 * g, 16)), k1 = *(const f32x4*)(kvs + dc * DH + min(8 * g + 4, 16));
        const bool v0 = d < DH && g < 3, v1 = d < DH && g < 2;
        u32x4 u = {v0 ? pack2(k0[0], k0[1]) : 0u, v0 ? pack2(k0[2], k0[3]) : 0u, v1 ? pack2(k1[0], k1[1]) : 0u, v1 ? pack2(k1[2], k1[3]) : 0u};
        yk[t] = __builtin_bit_cast(bf16x8, u);
      }
      WAIT_L(0);
      char* dst[2];
#pragma unroll
      for (int t = 0; t < 2; ++t) {
        const int kk = ph * DH + 16 * t + 4 * g;
        dst[t] = (char*)wp + (size_t)((pj * 20 + (kk >> 5)) * 5 + (sw ? 3 : 0)) * 1024 + (16 * ((kk & 31) >> 3) + l15) * 16 + ((kk & 7) >> 2) * 8;
      }
#pragma unroll
      for (int c = 0; c < 3; ++c) {
        const bf16x8 wa = cat8(al[c], ah[c]);
#pragma unroll
        for (int t = 0; t < 2; ++t) {
          const f32x4 z4 = {0.f, 0.f, 0.f, 0.f};
          f32x4 da = __builtin_amdgcn_mfma_f32_16x16x32_bf16(yk[t], wa, z4, 0, 0, 0);
          unsigned long long va = (unsigned long long)pack2(da[0], da[1]) | ((unsigned long long)pack2(da[2], da[3]) << 32);
          if (16 * t + 4 * g < DH && (c < 2 || !sw))
            __hip_atomic_store((unsigned long long*)(dst[t] + c * 1024), va, __ATOMIC_RELAXED, __HIP_MEMORY_SCOPE_AGENT);
        }
      }
      __builtin_amdgcn_sched_barrier(0);
      ldU(seof(q), I0{}, I0{});
      WAIT_VS(9);
      if (lane == 0) __hip_atomic_store(flags + (pj * 32 + ph) * 2 + sw, 1u, __ATOMIC_RELAXED, __HIP_MEMORY_SCOPE_AGENT);
      __builtin_amdgcn_s_setprio(1);
      __builtin_amdgcn_sched_barrier(0);
    } else {
      ldU(seof(q), I0{}, I0{});
    }
    ldU(seof(q + 1), I1{}, I0{});
    ldU(seof(q + 2), I2{}, I0{});
    zacc();
#pragma nounroll
    for (int gi = 1; gi < 4; ++gi) {
      WAIT_VS(18); unit(I0{}, I0{}, I1{}, seof(q + 3));
      WAIT_VS(18); unit(I1{}, I0{}, I1{}, seof(q + 4));
      WAIT_VS(18); unit(I2{}, I0{}, I1{}, seof(q + 5));
      q += 3;
    }
    WAIT_VS(18); unit(I0{}, I0{}, I0{}, 0);
    WAIT_VS(9); unit(I1{}, I0{}, I0{}, 0);
    WAIT_VS(0); unit(I2{}, I0{}, I0{}, 0);
  } else {
    __builtin_amdgcn_s_setprio(2);
    const int k = wid - 4;
    char* sm = smem + wid * K2_REG;
    {
      const char* zb = (const char*)kvp + (size_t)((8 * k + lane / 5) * KVSZ + DH * DH + 4 * (lane % 5)) * 4;
      if (lane < 40) {
#pragma unroll
        for (int r = 0; r < 8; ++r) glds16(zb + (size_t)r * (NH * KVSZ * 4), sm + K2_S_ZL + r * 640);
      }
      __builtin_amdgcn_sched_barrier(0);
    }
    const char* pqb[4];
#pragma unroll
    for (int i = 0; i < 4; ++i) pqb[i] = (const char*)pq + (size_t)((rb * 4 + ((i + wid) & 3)) * 20 + 5 * k) * 1024;
    bf16x8 pf[5][4];
#pragma unroll
    for (int i = 0; i < 4; ++i) pf[0][i] = gld128a<0>(pqb[i], lane16);
#pragma unroll
    for (int i = 0; i < 4; ++i) pf[1][i] = gld128a<1024>(pqb[i], lane16);
#pragma unroll
    for (int i = 0; i < 4; ++i) pf[2][i] = gld128a<2048>(pqb[i], lane16);
#pragma unroll
    for (int i = 0; i < 4; ++i) pf[3][i] = gld128a<3072>(pqb[i], lane16);
#pragma unroll
    for (int i = 0; i < 4; ++i) pf[4][i] = gld128a<0>(pqb[i] + 4096, lane16);
    __builtin_amdgcn_sched_barrier(0);
    BAR();
    float* zs = (float*)(sm + K2_S_ZS);
    float* rden = (float*)(sm + K2_S_RD);
    WAIT_VS(0);
#pragma unroll
    for (int u = 0; u < 5; ++u)
#pragma unroll
      for (int i = 0; i < 4; ++i) asm volatile("" : "+a"(pf[u][i]));
    __builtin_amdgcn_sched_barrier(0);
    {
      const int lane = lid();
      const int l1 = min(lane, 39);
      f32x4 z0 = *(const f32x4*)(sm + K2_S_ZL + l1 * 16);
#pragma unroll
      for (int r = 1; r < 8; ++r) z0 += *(const f32x4*)(sm + K2_S_ZL + r * 640 + l1 * 16);
#pragma unroll
      for (int e = 0; e < 4; ++e) z0[e] = rbf(z0[e]);
      *(f32x4*)(zs + 4 * lane) = z0;
    }
    WAIT_L(0);
    __builtin_amdgcn_wave_barrier();
    {
      const int lq = lid();
      const int g = lq >> 4, l15 = lq & 15;
      f32x4 dn[4];
#pragma unroll
      for (int i = 0; i < 4; ++i) dn[i] = (f32x4){0.f, 0.f, 0.f, 0.f};
#pragma unroll
      for (int u = 0; u < 5; ++u) {
        f32x4 z0 = *(const f32x4*)(zs + 32 * u + 8 * g), z1 = *(const f32x4*)(zs + 32 * u + 8 * g + 4);
        float zv[8] = {z0[0], z0[1], z0[2], z0[3], z1[0], z1[1], z1[2], z1[3]};
        const int k0 = 32 * u + 8 * g;
        const int h0 = k0 / DH, lim = (h0 + 1) * DH - k0;
        const bool m0_ = (h0 == l15), m1_ = (h0 + 1 == l15);
        u32x4 zm;
#pragma unroll
        for (int e = 0; e < 4; ++e) {
          bool m = (2 * e < lim) ? m0_ : m1_;
          zm[e] = m ? pack2(zv[2 * e], zv[2 * e + 1]) : 0u;
        }
        bf16x8 zz = __builtin_bit_cast(bf16x8, zm);
#pragma unroll
        for (int i = 0; i < 4; ++i) dn[i] = __builtin_amdgcn_mfma_f32_16x16x32_bf16(zz, pf[u][i], dn[i], 0, 0, 0);
        __builtin_amdgcn_sched_barrier(0);
      }
      if (g < 2) {
#pragma unroll
        for (int i = 0; i < 4; ++i) {
          f32x4 r;
#pragma unroll
          for (int e = 0; e < 4; ++e) r[e] = __builtin_amdgcn_rcpf(rbf(rbf(dn[i][e]) + 1e-6f));
          *(f32x4*)(rden + (i * 16 + l15) * 8 + 4 * g) = r;
        }
      }
      __builtin_amdgcn_s_waitcnt(0xc07f);
      __builtin_amdgcn_wave_barrier();
#pragma unroll
      for (int u = 0; u < 5; ++u) {
        const int k0 = 32 * u + 8 * g;
        const int h0 = k0 / DH;
        const int lim = (h0 + 1) * DH - k0;
#pragma unroll
        for (int i = 0; i < 4; ++i) {
          const float* rp = rden + (i * 16 + l15) * 8 + h0;
          float r0 = rp[0], r1 = rp[1];
          u32x4 uu = __builtin_bit_cast(u32x4, pf[u][i]);
          u32x4 o;
#pragma unroll
          for (int e = 0; e < 4; ++e) {
            float r = (2 * e < lim) ? r0 : r1;
            o[e] = pack2(bflo(uu[e]) * r, bfhi(uu[e]) * r);
          }
          pf[u][i] = __builtin_bit_cast(bf16x8, o);
        }
        __builtin_amdgcn_sched_barrier(0);
      }
      __builtin_amdgcn_s_waitcnt(0xc07f);
      __builtin_amdgcn_sched_barrier(0);
    }
    auto park = [&](int reg) {
      char* myr = smem + reg * K2_REG + lane16;
#pragma unroll
      for (int i = 0; i < 4; ++i)
#pragma unroll
        for (int j = 0; j < 5; ++j) *(f32x4*)(myr + (((i + wid) & 3) * 5 + (j + 3) % 5) * 1024) = acc[i][j];
      __builtin_amdgcn_sched_barrier(0);
    };
    if (k >= 2) {
      __builtin_amdgcn_s_setprio(1);
      const int q = 42 + 3 * k;
      char* st = sm + lane16;
#pragma unroll
      for (int u = 0; u < 5; ++u)
#pragma unroll
        for (int i = 0; i < 4; ++i) *(bf16x8*)(st + (u * 4 + i) * 1024) = pf[u][i];
      __builtin_amdgcn_sched_barrier(0);
      ldU(seof(q), I0{}, I1{});
      ldU(seof(q + 1), I1{}, I1{});
      ldU(seof(q + 2), I2{}, I1{});
      zacc();
      WAIT_VS(18); unit(I0{}, I1{}, I0{}, 0);
      WAIT_VS(9); unit(I1{}, I1{}, I0{}, 0);
      WAIT_VS(0); unit(I2{}, I1{}, I0{}, 0);
      park(k);
#pragma unroll
      for (int u = 0; u < 5; ++u)
#pragma unroll
        for (int i = 0; i < 4; ++i) pf[u][i] = *(const bf16x8*)(st + (u * 4 + i) * 1024);
      __builtin_amdgcn_sched_barrier(0);
    }
    __builtin_amdgcn_s_setprio(3);
    unsigned f[3];
    {
      const unsigned* fp = flags + (cb * 32 + 8 * k) * 2;
      const unsigned fo = (lane16 >> 2) & 60u;
      f[0] = gld32_sc1(fp, fo);
      __builtin_amdgcn_s_sleep(8);
      f[1] = gld32_sc1(fp, fo);
      __builtin_amdgcn_s_sleep(8);
      for (;;) {
        f[2] = gld32_sc1(fp, fo);
        WAIT_VS(2);
        if (__all(f[0] == 1u)) break;
        __builtin_amdgcn_s_sleep(8);
        f[0] = gld32_sc1(fp, fo);
        WAIT_VS(2);
        if (__all(f[1] == 1u)) break;
        __builtin_amdgcn_s_sleep(8);
        f[1] = gld32_sc1(fp, fo);
        WAIT_VS(2);
        if (__all(f[2] == 1u)) break;
        __builtin_amdgcn_s_sleep(8);
      }
    }
    __builtin_amdgcn_sched_barrier(0);
    const char* wpb = (const char*)wp + (size_t)(cb * 20 + 5 * k) * 5120 + 2048;
    auto ldS = [&](auto U) {
      sfor<5>([&](auto J) {
        bf[U.value % 3][J.value] = gld128_sc1<((J.value + 3) % 5 - 2) * 1024>(wpb + U.value * 5120, lane16);
      });
      __builtin_amdgcn_sched_barrier(0);
    };
    ldS(I0{}); ldS(I1{}); ldS(I2{});
    zacc();
    __builtin_amdgcn_sched_barrier(0);
    sfor<5>([&](auto UU) {
      constexpr int u = UU.value;
      if constexpr (u <= 2) WAIT_VS(10); else if constexpr (u == 3) WAIT_VS(5); else WAIT_VS(0);
      if constexpr (u == 0) asm volatile("" :: "v"(f[0]), "v"(f[1]), "v"(f[2]));
      sfor<5>([&](auto J) {
        sfor<4>([&](auto I) {
          constexpr int i = I.value, j = J.value;
          acc[i][j] = __builtin_amdgcn_mfma_f32_16x16x32_bf16(bf[u % 3][j], pf[u][i], acc[i][j], 0, 0, 0);
          __builtin_amdgcn_sched_barrier(0);
        });
      });
      if constexpr (u + 3 < 5) ldS(std::integral_constant<int, u + 3>{});
    });
    park(wid);
  }

  int ln;
  asm volatile("v_mbcnt_lo_u32_b32 %0, -1, 0\n\tv_mbcnt_hi_u32_b32 %0, -1, %0" : "=v"(ln));
  const int g2 = ln >> 4;
  const unsigned orow = (unsigned)(m0 + (wid & 3) * 16 + (ln & 15));
  u32x2 xv[3];
#pragma unroll
  for (int j = 0; j < 3; ++j) xv[j] = *(const u32x2*)(x + (size_t)orow * D_ + n0 + ((j + 3 * hi) % 5) * 16 + 4 * g2);
  __syncthreads();
  if (wid < 4) {
    char* myr = smem + wid * K2_REG + ln * 16;
#pragma unroll
    for (int i = 0; i < 4; ++i)
#pragma unroll
      for (int j = 0; j < 5; ++j) {
        const int slot = ((i + wid) & 3) * 5 + j;
        if (wid >= 2) acc[i][j] += *(const f32x4*)(myr + slot * 1024);
        *(f32x4*)(myr + slot * 1024) = acc[i][j];
      }
  }
  __syncthreads();
  f32x4 ys[3], ss[3];
#pragma unroll
  for (int j = 0; j < 3; ++j) { ys[j] = (f32x4){0.f, 0.f, 0.f, 0.f}; ss[j] = (f32x4){0.f, 0.f, 0.f, 0.f}; }
#pragma unroll
  for (int w = 0; w < 8; ++w) {
    const char* src = smem + w * K2_REG + ln * 16 + (wid & 3) * 5 * 1024;
#pragma unroll
    for (int j = 0; j < 3; ++j) {
      if (j == 2 && hi) continue;
      f32x4 a = *(const f32x4*)(src + ((j + 3 * hi) % 5) * 1024);
      if (w < 4) ys[j] += a; else ss[j] += a;
    }
  }
#pragma unroll
  for (int j = 0; j < 3; ++j) {
    if (j == 2 && hi) continue;
    size_t o = (size_t)orow * D_ + n0 + ((j + 3 * hi) % 5) * 16 + 4 * g2;
    float xs[4] = {bflo(xv[j][0]), bfhi(xv[j][0]), bflo(xv[j][1]), bfhi(xv[j][1])};
    float r[4];
#pragma unroll
    for (int e = 0; e < 4; ++e) {
      float y = rbf(ys[j][e]);
      float gt = rbf(y * __builtin_amdgcn_rcpf(1.0f + __expf(-y)));
      float sv = rbf(ss[j][e]);
      r[e] = xs[e] + rbf(sv * gt);
    }
    u32x2 ov = {pack2(r[0], r[1]), pack2(r[2], r[3])};
    *(u32x2*)(out + o) = ov;
  }
}

extern "C" void kernel_launch(void* const* d_in, const int* in_sizes, int n_in, void* d_out, int out_size,
                              void* d_ws, size_t ws_size, hipStream_t stream) {
  const u16* x = (const u16*)d_in[0];
  const u16* wqkv = (const u16*)d_in[1];
  const u16* wo = (const u16*)d_in[2];
  const u16* wg = (const u16*)d_in[4];
  const u16* wu = (const u16*)d_in[5];
  const u16* wd = (const u16*)d_in[6];
  char* ws = (char*)d_ws;
  u16* pq = (u16*)(ws);
  u16* h = (u16*)(ws + (4 << 20));
  float* kvp = (float*)(ws + (12 << 20));
  u16* wp = (u16*)(ws + (13 << 20));
  unsigned* flags = (unsigned*)(ws + (14 << 20));
  u16* wdt = (u16*)(ws + (16 << 20));
  (void)hipFuncSetAttribute((const void*)k1_kernel, hipFuncAttributeMaxDynamicSharedMemorySize, K1_LDS);
  (void)hipFuncSetAttribute((const void*)k2_kernel, hipFuncAttributeMaxDynamicSharedMemorySize, K2_LDS);
  k1_kernel<<<256, 512, K1_LDS, stream>>>(x, wqkv, wg, wu, pq, h, kvp, flags, wd, wo, wdt);
  k2_kernel<<<256, 512, K2_LDS, stream>>>(x, h, wdt, pq, kvp, wo, wp, flags, (u16*)d_out);
}
```

```cpp
#include <hip/hip_runtime.h>
#include <hip/hip_bf16.h>
#include <stdint.h>
#include <utility>

typedef unsigned short u16;
typedef __attribute__((ext_vector_type(8))) short bf16x8;
typedef __attribute__((ext_vector_type(4))) short s16x4;
typedef __attribute__((ext_vector_type(4))) float f32x4;
typedef __attribute__((ext_vector_type(2))) __bf16 bf2;
typedef __attribute__((ext_vector_type(4))) unsigned u32x4;
typedef __attribute__((ext_vector_type(2))) unsigned u32x2;
#define LAS __attribute__((address_space(3)))

constexpr int S_ = 2048, D_ = 640, F_ = 1728, NH = 32, DH = 20;
constexpr int KVSZ = DH * DH + DH;

__device__ __forceinline__ unsigned pack2(float a, float b) {
  bf2 v = {(__bf16)a, (__bf16)b};
  return __builtin_bit_cast(unsigned, v);
}
__device__ __forceinline__ float rbf(float a) { return (float)(__bf16)a; }
__device__ __forceinline__ float bflo(unsigned u) { return __uint_as_float(u << 16); }
__device__ __forceinline__ float bfhi(unsigned u) { return __uint_as_float(u & 0xffff0000u); }
__device__ __forceinline__ float dot_ones(unsigned a, float c) {
  bf2 one = {(__bf16)1.0f, (__bf16)1.0f};
  return __builtin_amdgcn_fdot2_f32_bf16(__builtin_bit_cast(bf2, a), one, c, false);
}
__device__ __forceinline__ float dot_self(unsigned a, float c) {
  bf2 v = __builtin_bit_cast(bf2, a);
  return __builtin_amdgcn_fdot2_f32_bf16(v, v, c, false);
}
typedef __attribute__((ext_vector_type(2))) float f32x2;
__device__ __forceinline__ f32x2 up2(unsigned p) { return (f32x2){bflo(p), bfhi(p)}; }
__device__ __forceinline__ unsigned pk2(f32x2 v) { return pack2(v[0], v[1]); }
__device__ __forceinline__ f32x2 rbf2(f32x2 v) {
  unsigned p = pk2(v); asm("" : "+v"(p)); return up2(p);
}
__device__ __forceinline__ f32x2 exp2_2(f32x2 a) { return (f32x2){__builtin_amdgcn_exp2f(a[0]), __builtin_amdgcn_exp2f(a[1])}; }
constexpr float LOG2E = 1.44269504088896340736f;
__device__ __forceinline__ void rbf4(f32x2 (&v)[4]) {
  unsigned p[4];
#pragma unroll
  for (int n = 0; n < 4; ++n) p[n] = pk2(v[n]);
  asm("" : "+v"(p[0]), "+v"(p[1]), "+v"(p[2]), "+v"(p[3]));
#pragma unroll
  for (int n = 0; n < 4; ++n) v[n] = up2(p[n]);
}
__device__ __forceinline__ void rbf4(f32x2 (&v)[2]) {
  unsigned p0 = pk2(v[0]), p1 = pk2(v[1]);
  asm("" : "+v"(p0), "+v"(p1));
  v[0] = up2(p0); v[1] = up2(p1);
}
__device__ __forceinline__ void phi4(f32x2 (&t)[4], unsigned (&o)[4]) {
  rbf4(t);
  f32x2 ex[4], p[4];
#pragma unroll
  for (int n = 0; n < 4; ++n) ex[n] = t[n] * LOG2E;
#pragma unroll
  for (int n = 0; n < 4; ++n) ex[n] = exp2_2(ex[n]);
#pragma unroll
  for (int n = 0; n < 4; ++n) p[n] = t[n] + 1.0f;
#pragma unroll
  for (int n = 0; n < 4; ++n) p[n] = (f32x2){t[n][0] > 0.f ? p[n][0] : ex[n][0], t[n][1] > 0.f ? p[n][1] : ex[n][1]};
  rbf4(p);
#pragma unroll
  for (int n = 0; n < 4; ++n) p[n] = p[n] + 1e-6f;
#pragma unroll
  for (int n = 0; n < 4; ++n) o[n] = pk2(p[n]);
}
__device__ __forceinline__ void glds16(const void* g, void* l) {
  __builtin_amdgcn_global_load_lds((const unsigned*)g, (unsigned*)l, 16, 0, 0);
}
template <class F, int... I> __device__ __forceinline__ void sfor_impl(F&& f, std::integer_sequence<int, I...>) {
  (f(std::integral_constant<int, I>{}), ...);
}
template <int N, class F> __device__ __forceinline__ void sfor(F&& f) { sfor_impl(f, std::make_integer_sequence<int, N>{}); }
#define WAIT_V(n) asm volatile("s_waitcnt vmcnt(" #n ")" ::: "memory")
#define WAIT_VS(n) do { WAIT_V(n); __builtin_amdgcn_sched_barrier(0); } while (0)
#define BAR() __builtin_amdgcn_s_barrier()
template <int OFF> __device__ __forceinline__ s16x4 tr_rd(unsigned a) {
  s16x4 r; asm volatile("ds_read_b64_tr_b16 %0, %1 offset:%2" : "=&v"(r) : "v"(a), "i"(OFF) : "memory"); return r;
}
template <int OFF> __device__ __forceinline__ bf16x8 lds_rd128(unsigned a) {
  bf16x8 r; asm volatile("ds_read_b128 %0, %1 offset:%2" : "=&v"(r) : "v"(a), "i"(OFF) : "memory"); return r;
}
template <int OFF> __device__ __forceinline__ bf16x8 gld128(const void* sbase, unsigned voff) {
  bf16x8 r; asm volatile("global_load_dwordx4 %0, %1, %2 offset:%3" : "=&v"(r) : "v"(voff), "s"(sbase), "i"(OFF) : "memory"); return r;
}
template <int OFF> __device__ __forceinline__ bf16x8 gld128a(const void* sbase, unsigned voff) {
  bf16x8 r; asm volatile("global_load_dwordx4 %0, %1, %2 offset:%3" : "=&a"(r) : "v"(voff), "s"(sbase), "i"(OFF) : "memory"); return r;
}
__device__ __forceinline__ bf16x8 cat8(s16x4 a, s16x4 b) { return __builtin_shufflevector(a, b, 0, 1, 2, 3, 4, 5, 6, 7); }
#define WAIT_L(n) do { asm volatile("s_waitcnt lgkmcnt(" #n ")" ::: "memory"); __builtin_amdgcn_sched_barrier(0); } while (0)
__device__ __forceinline__ unsigned lds_addr(const void* p) { return (unsigned)(size_t)(LAS const char*)p; }
__device__ __forceinline__ float phi_bf(float t) {
  float p = t > 0.f ? rbf(t + 1.0f) : rbf(__expf(t));
  return rbf(p + 1e-6f);
}

constexpr int K1_ABYTES = 256 * 64 * 2;
constexpr int K1_BBYTES = 64 * 192 * 2;
constexpr int K1_OFF_B = 3 * K1_ABYTES;
constexpr int K1_OFF_MISC = K1_OFF_B + 2 * K1_BBYTES;
constexpr int K1_OFF_DUMMY = K1_OFF_MISC;
constexpr int K1_OFF_MEAN = K1_OFF_DUMMY + 1024;
constexpr int K1_OFF_CSF = K1_OFF_MEAN + 2048;
constexpr int K1_LDS = K1_OFF_CSF + 768;
constexpr int K1_OFF_RS = K1_ABYTES;
constexpr int K1_OFF_CS = 2 * K1_ABYTES;

template <int ROLE>
__device__ __forceinline__ void k1_body(const u16* __restrict__ x, const u16* __restrict__ w0,
                                        const u16* __restrict__ w1, u16* __restrict__ outp,
                                        float* __restrict__ kvp, int rb, int cgl,
                                        const char* __restrict__ wop, char* smem) {
  constexpr int BN = ROLE == 2 ? 192 : 160;
  constexpr int NB = BN / 16;
  constexpr int WM = ROLE == 1 ? 8 : 4;
  constexpr int WN = 8 / WM;
  constexpr int MT = 16 / WM;
  constexpr int NT = NB / WN;
  constexpr int BPIECES = 64 * BN * 2 / 1024;
  constexpr int LDW = ROLE == 2 ? F_ : 3 * D_;
  constexpr int RBI = ROLE == 2 ? 192 : ROLE == 0 ? 320 : 160;
  constexpr int CPR = RBI / 16;
  constexpr int PPI = 64 * RBI / 1024;
  constexpr int IMGB = 64 * RBI;

  const int tid = threadIdx.x;
  const int wid = __builtin_amdgcn_readfirstlane(tid >> 6);
  const int lane = tid & 63;
  const int g = lane >> 4, l15 = lane & 15;
  const int wm = wid % WM, wn = wid / WM;
  const int m0 = rb * 256;
  const int CSOFF = 2 * wn;

  const char* abase = (const char*)(x + (size_t)m0 * D_);
  const unsigned aoffg = (unsigned)(((wid * 8 + (lane >> 3)) * D_ + (((lane & 7) ^ (lane >> 3)) * 8)) * 2);
  const char* bptr[3];
#pragma unroll
  for (int i = 0; i < 3; ++i) {
    int p = wid + 8 * i;
    if (p >= BPIECES) p = wid;
    const int img = p / PPI, c = (p - img * PPI) * 64 + lane;
    const int rho = c / CPR, chp = c - rho * CPR;
    const int k = ROLE == 1 ? (rho ^ (((rho >> 3) & 1) << 2)) : rho;
    const int ch = ROLE == 1 ? chp : (chp ^ (2 * ((rho >> 3) & 1)));
    const u16* wp;
    if (ROLE == 0) wp = w0 + cgl * 160;
    else if (ROLE == 1) wp = w0 + (img ? 2 * D_ : D_) + cgl * 80;
    else wp = (img ? w1 : w0) + cgl * 96;
    bptr[i] = (const char*)(wp + (size_t)k * LDW + ch * 8);
  }
  auto loadA = [&](int t, int slot, int i) {
    glds16(abase + (size_t)(i * 64 * D_ * 2 + t * 128) + aoffg, smem + slot * K1_ABYTES + (wid + 8 * i) * 1024);
  };
  auto loadB = [&](int t, int slot, int i) {
    if (wid + 8 * i < BPIECES) {
      glds16(bptr[i] + (size_t)t * (64 * LDW * 2), smem + K1_OFF_B + slot * K1_BBYTES + (wid + 8 * i) * 1024);
    }
  };
  auto stageA = [&](int t, int slot) {
#pragma unroll
    for (int i = 0; i < 4; ++i) loadA(t, slot, i);
  };
  auto stageB = [&](int t, int slot) {
#pragma unroll
    for (int i = 0; i < 3; ++i) loadB(t, slot, i);
  };

  const char* pfo = wop + (size_t)((blockIdx.x * 8 + wid) % 800) * 1024 + lane * 16;
  stageA(0, 0);
  stageB(0, 0);
  stageA(1, 1);

  const int arow = wm * (MT * 16) + l15;
  const unsigned lbase = lds_addr(smem);
  const unsigned aoff0 = lbase + arow * 128 + ((g ^ (lane & 7)) * 16);
  const int bq = l15 >> 2, bp = lane & 3;
  constexpr int NBJ = ROLE == 2 ? 3 : ROLE == 0 ? 5 : 2;
  unsigned bj[NBJ];
  if (ROLE == 1) {
    bj[0] = lbase + K1_OFF_B + (8 * g + bq + 4 * (g & 1)) * RBI + bp * 8;
    bj[1] = lbase + K1_OFF_B + (8 * g + bq + 4 * ((g & 1) ^ 1)) * RBI + bp * 8;
  } else {
#pragma unroll
    for (int j = 0; j < NBJ; ++j) bj[j] = lbase + K1_OFF_B + (8 * g + bq) * RBI + (((wn * NBJ + j) ^ (g & 1)) * 32) + bp * 8;
  }

  f32x4 acc[MT][NT];
#pragma unroll
  for (int i = 0; i < MT; ++i)
#pragma unroll
    for (int j = 0; j < NT; ++j) acc[i][j] = (f32x4){0.f, 0.f, 0.f, 0.f};
  float rs[MT], rq[MT], cs[NT];
#pragma unroll
  for (int i = 0; i < MT; ++i) { rs[i] = 0.f; rq[i] = 0.f; }
#pragma unroll
  for (int j = 0; j < NT; ++j) cs[j] = 0.f;

  bf16x8 xf[2][MT];
  s16x4 wl[2][NT], wh[2][NT];
  auto rd = [&](auto S, unsigned soa, unsigned sob) {
    constexpr int s = S.value;
    const unsigned aa = (aoff0 ^ (s * 64)) + soa;
    sfor<MT>([&](auto I) { xf[s][I.value] = lds_rd128<I.value * 2048>((I.value & 1) ? aa - wn * 2048 : aa + wn * 2048); });
    unsigned bs[NBJ];
#pragma unroll
    for (int j = 0; j < NBJ; ++j) bs[j] = bj[j] + sob;
    sfor<NT>([&](auto J) {
      constexpr int j = J.value;
      if constexpr (ROLE == 1) {
        constexpr int off = (j / 5) * IMGB + s * 32 * RBI + (j % 5) * 32;
        wl[s][j] = tr_rd<off>(bs[0]);
        wh[s][j] = tr_rd<off>(bs[1]);
      } else {
        constexpr int off = (j / NBJ) * IMGB + s * 32 * RBI;
        wl[s][j] = tr_rd<off>(bs[j % NBJ]);
        wh[s][j] = tr_rd<off + 4 * RBI>(bs[j % NBJ]);
      }
    });
  };
  auto rd1 = [&](auto S, auto R, unsigned aa, const unsigned* bs) {
    constexpr int s = S.value, r = R.value;
    if constexpr (r < MT) {
      xf[s][r] = lds_rd128<r * 2048>((r & 1) ? aa - wn * 2048 : aa + wn * 2048);
    } else {
      constexpr int j = (r - MT) / 2, hi = (r - MT) & 1;
      if constexpr (ROLE == 1) {
        constexpr int off = (j / 5) * IMGB + s * 32 * RBI + (j % 5) * 32;
        if constexpr (hi) wh[s][j] = tr_rd<off>(bs[1]); else wl[s][j] = tr_rd<off>(bs[0]);
      } else {
        constexpr int off = (j / NBJ) * IMGB + s * 32 * RBI;
        if constexpr (hi) wh[s][j] = tr_rd<off + 4 * RBI>(bs[j % NBJ]); else wl[s][j] = tr_rd<off>(bs[j % NBJ]);
      }
    }
  };
  auto mm = [&](auto S, auto LDX, int t, int nslot, bool do_cs, auto RSX, unsigned soa, unsigned sob) {
    constexpr int s = S.value, LD = LDX.value, RS = RSX.value;
    constexpr int NRDX = MT + 2 * NT;
    const unsigned aa = (aoff0 ^ ((RS & 1) * 64)) + soa;
    unsigned bs[NBJ];
#pragma unroll
    for (int j = 0; j < NBJ; ++j) bs[j] = bj[j] + sob;
    bf16x8 wf[NT];
#pragma unroll
    for (int j = 0; j < NT; ++j) wf[j] = cat8(wl[s][j], wh[s][j]);
    constexpr int NM = MT * NT;
    constexpr int ND = (WN == 2) ? (MT / 2) * 8 : MT * 8;
    sfor<NM>([&](auto X) {
      constexpr int xi = X.value, i = xi / NT, j = xi % NT;
      if (ROLE == 1) acc[i][j] = __builtin_amdgcn_mfma_f32_16x16x32_bf16(xf[s][i], wf[j], acc[i][j], 0, 0, 0);
      else acc[i][j] = __builtin_amdgcn_mfma_f32_16x16x32_bf16(wf[j], xf[s][i], acc[i][j], 0, 0, 0);
      constexpr int d0 = xi * ND / NM, d1 = (xi + 1) * ND / NM;
      sfor<d1 - d0>([&](auto Y) {
        constexpr int d = d0 + Y.value;
        constexpr int NTL = ND / 8;
        constexpr int q = (d >> 1) % NTL, e = (d >> 1) / NTL;
        constexpr int ii = (WN == 2) ? 2 * q : q;
        unsigned u = __builtin_bit_cast(u32x4, xf[s][ii])[e];
        if (d & 1) rq[q] = dot_self(u, rq[q]); else rs[q] = dot_ones(u, rs[q]);
      });
      if constexpr (RS < 2) {
        constexpr int NMR = (NM * 3) / 4;
        constexpr int r0 = xi < NMR ? xi * NRDX / NMR : NRDX, r1 = xi + 1 < NMR ? (xi + 1) * NRDX / NMR : NRDX;
        sfor<r1 - r0>([&](auto Y) { rd1(std::integral_constant<int, RS & 1>{}, std::integral_constant<int, r0 + Y.value>{}, aa, bs); });
      }
      constexpr int P = NM / 4;
      if constexpr (LD == 0 && (xi == 1 || xi == 1 + P || xi == 1 + 2 * P)) { if (t + 1 < 10) loadB(t + 1, (t + 1) & 1, (xi - 1) / P); else if (xi == 1) glds16(pfo, smem + K1_OFF_DUMMY); }
      if constexpr (LD == 0 && xi == 1 + 3 * P) { if (t + 2 < 10) loadA(t + 2, nslot, 0); }
      if constexpr (LD == 1 && (xi == 1 || xi == 1 + P || xi == 1 + 2 * P)) {
        if (t + 2 < 10) loadA(t + 2, nslot, 1 + (xi - 1) / P);
      }
      __builtin_amdgcn_sched_barrier(0);
    });
    if (do_cs) {
#pragma unroll
      for (int j = 0; j < NT; ++j) {
        u32x4 u = __builtin_bit_cast(u32x4, wf[j]);
#pragma unroll
        for (int e = 0; e < 4; ++e) cs[j] = dot_ones(u[e], cs[j]);
      }
    }
  };
  using I0 = std::integral_constant<int, 0>; using I1 = std::integral_constant<int, 1>; using I2 = std::integral_constant<int, 2>;
  constexpr int NRD = MT + 2 * NT;

  int aslot = 0;
  {
    bool prev_cs = (CSOFF % WM) == wm;
    {
      WAIT_V(4);
      BAR();
      rd(I0{}, 0u, 0u);
      stageB(1, 1);
      loadA(2, 2, 0);
      WAIT_L(0);
      mm(I0{}, I1{}, 0, 2, prev_cs, I1{}, 0u, 0u);
      WAIT_L(0);
      aslot = 1;
    }
#pragma nounroll
    for (int t = 1; t < 10; ++t) {
      if (t < 9) WAIT_V(4); else WAIT_V(0);
      BAR();
      const int nslot = aslot == 0 ? 2 : aslot - 1;
      const unsigned soa = aslot * K1_ABYTES, sob = (t & 1) * K1_BBYTES;
      aslot = aslot == 2 ? 0 : aslot + 1;
      const bool do_cs = ((t + CSOFF) % WM) == wm;
      mm(I1{}, I0{}, t, nslot, prev_cs, I0{}, soa, sob);
      WAIT_L(0);
      mm(I0{}, I1{}, t, nslot, do_cs, I1{}, soa, sob);
      WAIT_L(0);
      prev_cs = do_cs;
    }
    mm(I1{}, I2{}, 10, 0, prev_cs, I2{}, 0u, 0u);
  }

  float* rsum = (float*)(smem + K1_OFF_RS);
  float* csum = (float*)(smem + K1_OFF_CS);
  float* smean = (float*)(smem + K1_OFF_MEAN);
  float* srsd = smean + 256;
  float* scs = (float*)(smem + K1_OFF_CSF);
#pragma unroll
  for (int q = 0; q < (WN == 2 ? MT / 2 : MT); ++q) {
    int r = wm * (MT * 16) + ((WN == 2) ? ((2 * q) ^ wn) : q) * 16 + l15;
    float2 v = {rs[q], rq[q]};
    *(float2*)(rsum + (g * 256 + r) * 2) = v;
  }
#pragma unroll
  for (int j = 0; j < NT; ++j) {
    int nb = (ROLE == 2) ? ((j < 3) ? (wn * 3 + j) : (6 + wn * 3 + (j - 3))) : (wn * NT + j);
    csum[(wm * 4 + g) * 192 + nb * 16 + l15] = cs[j];
  }
  WAIT_L(0);
  BAR();
  if (tid < 256) {
    float a = 0.f, b = 0.f;
#pragma unroll
    for (int w = 0; w < 4; ++w) { float2 v = *(const float2*)(rsum + (w * 256 + tid) * 2); a += v.x; b += v.y; }
    float mean = a * (1.0f / D_);
    float var = fmaxf(b * (1.0f / D_) - mean * mean, 0.f);
    float sd = rbf(sqrtf(rbf(rbf(var) + 1e-5f)));
    smean[tid] = rbf(mean);
    srsd[tid] = 1.0f / sd;
  } else if (tid - 256 < BN) {
    int n = tid - 256;
    float a = 0.f;
#pragma unroll
    for (int w = 0; w < WM * 4; ++w) a += csum[w * 192 + n];
    scs[n] = a;
  }
  WAIT_L(0);
  BAR();

  if (ROLE == 0) {
    constexpr int PITCH = 336;
    float mean[MT], rsd[MT];
    f32x4 c4[NT];
#pragma unroll
    for (int i = 0; i < MT; ++i) { int r = wm * 64 + (i ^ wn) * 16 + l15; mean[i] = smean[r]; rsd[i] = srsd[r]; }
#pragma unroll
    for (int j = 0; j < NT; ++j) c4[j] = *(const f32x4*)(scs + (wn * NT + j) * 16 + 4 * g);
    char* wb = smem + (wm * 64 + l15) * PITCH + (wn * NT * 16 + 4 * g) * 2;
    __builtin_amdgcn_sched_barrier(0);
#pragma unroll
    for (int ip = 0; ip < MT; ip += 2) {
#pragma unroll
      for (int j = 0; j < NT; ++j) {
        f32x2 t[4];
        unsigned o[4];
#pragma unroll
        for (int n = 0; n < 4; ++n) {
          const int i = ip + (n >> 1), e = 2 * (n & 1);
          t[n] = ((f32x2){acc[i][j][e], acc[i][j][e + 1]} - mean[i] * (f32x2){c4[j][e], c4[j][e + 1]}) * rsd[i];
        }
        phi4(t, o);
        *(u32x2*)(wb + (ip ^ wn) * 16 * PITCH + j * 32) = (u32x2){o[0], o[1]};
        *(u32x2*)(wb + ((ip + 1) ^ wn) * 16 * PITCH + j * 32) = (u32x2){o[2], o[3]};
      }
    }
    __syncthreads();
#pragma unroll
    for (int it = 0; it < 10; ++it) {
      int o = tid + 512 * it, blk = o >> 6, ln = o & 63;
      int rt = blk / 5, un = blk - rt * 5;
      u32x4 v = *(const u32x4*)(smem + (rt * 16 + (ln & 15)) * PITCH + (un * 4 + (ln >> 4)) * 16);
      *(u32x4*)(outp + ((size_t)((rb * 16 + rt) * 20 + cgl * 5 + un) * 64 + ln) * 8) = v;
    }
  } else if (ROLE == 2) {
    constexpr int PITCH = 208;
    float mean[MT], rsd[MT];
    f32x4 cg4[3], cu4[3];
#pragma unroll
    for (int i = 0; i < MT; ++i) { int r = wm * 64 + (i ^ wn) * 16 + l15; mean[i] = smean[r]; rsd[i] = srsd[r]; }
#pragma unroll
    for (int j = 0; j < 3; ++j) {
      cg4[j] = *(const f32x4*)(scs + (wn * 3 + j) * 16 + 4 * g);
      cu4[j] = *(const f32x4*)(scs + 96 + (wn * 3 + j) * 16 + 4 * g);
    }
    char* wb = smem + (wm * 64 + l15) * PITCH + (wn * 48 + 4 * g) * 2;
    __builtin_amdgcn_sched_barrier(0);
#pragma unroll
    for (int ip = 0; ip < MT; ip += 2) {
#pragma unroll
      for (int j = 0; j < 3; ++j) {
        f32x2 gv[4], uv[4], sg[4];
#pragma unroll
        for (int n = 0; n < 4; ++n) {
          const int i = ip + (n >> 1), e = 2 * (n & 1);
          gv[n] = ((f32x2){acc[i][j][e], acc[i][j][e + 1]} - mean[i] * (f32x2){cg4[j][e], cg4[j][e + 1]}) * rsd[i];
        }
#pragma unroll
        for (int n = 0; n < 4; ++n) {
          const int i = ip + (n >> 1), e = 2 * (n & 1);
          uv[n] = ((f32x2){acc[i][j + 3][e], acc[i][j + 3][e + 1]} - mean[i] * (f32x2){cu4[j][e], cu4[j][e + 1]}) * rsd[i];
        }
        rbf4(gv);
#pragma unroll
        for (int n = 0; n < 4; ++n) sg[n] = gv[n] * -LOG2E;
#pragma unroll
        for (int n = 0; n < 4; ++n) sg[n] = exp2_2(sg[n]);
        rbf4(uv);
#pragma unroll
        for (int n = 0; n < 4; ++n) sg[n] = sg[n] + 1.0f;
#pragma unroll
        for (int n = 0; n < 4; ++n) sg[n] = (f32x2){__builtin_amdgcn_rcpf(sg[n][0]), __builtin_amdgcn_rcpf(sg[n][1])};
        rbf4(sg);
#pragma unroll
        for (int n = 0; n < 4; ++n) sg[n] = sg[n] * uv[n];
        *(u32x2*)(wb + (ip ^ wn) * 16 * PITCH + j * 32) = (u32x2){pk2(sg[0]), pk2(sg[1])};
        *(u32x2*)(wb + ((ip + 1) ^ wn) * 16 * PITCH + j * 32) = (u32x2){pk2(sg[2]), pk2(sg[3])};
      }
    }
    __syncthreads();
#pragma unroll
    for (int it = 0; it < 6; ++it) {
      int o = tid + 512 * it, blk = o >> 6, ln = o & 63;
      int rt = blk / 3, un = blk - rt * 3;
      u32x4 v = *(const u32x4*)(smem + (rt * 16 + (ln & 15)) * PITCH + (un * 4 + (ln >> 4)) * 16);
      *(u32x4*)(outp + ((size_t)((rb * 16 + rt) * 54 + cgl * 3 + un) * 64 + ln) * 8) = v;
    }
  } else {
    bf16x8 fr[10];
    f32x2 mean[2][2], rsd[2][2];
    float cc[10];
#pragma unroll
    for (int i = 0; i < 2; ++i) {
      const int r = wid * 32 + i * 16 + 4 * g;
      const f32x4 m4 = *(const f32x4*)(smean + r), r4 = *(const f32x4*)(srsd + r);
      mean[i][0] = (f32x2){m4[0], m4[1]}; mean[i][1] = (f32x2){m4[2], m4[3]};
      rsd[i][0] = (f32x2){r4[0], r4[1]}; rsd[i][1] = (f32x2){r4[2], r4[3]};
    }
#pragma unroll
    for (int j = 0; j < 10; ++j) cc[j] = scs[j * 16 + l15];
    __builtin_amdgcn_sched_barrier(0);
#pragma unroll
    for (int j = 0; j < 10; ++j) {
      f32x2 t[4];
      unsigned o[4];
#pragma unroll
      for (int n = 0; n < 4; ++n) {
        const int i = n >> 1, e = n & 1;
        t[n] = ((f32x2){acc[i][j][2 * e], acc[i][j][2 * e + 1]} - mean[i][e] * cc[j]) * rsd[i][e];
      }
      if (j < 5) phi4(t, o);
      else {
#pragma unroll
        for (int n = 0; n < 4; ++n) o[n] = pk2(t[n]);
      }
      const u32x4 u = {o[0], o[1], o[2], o[3]};
      fr[j] = __builtin_bit_cast(bf16x8, u);
    }
    float* red = (float*)smem;
    float* myred = red + wid * (4 * KVSZ) + 80 * g + l15;
    const bf16x8 ones = {0x3F80, 0x3F80, 0x3F80, 0x3F80, 0x3F80, 0x3F80, 0x3F80, 0x3F80};
    int hv[5];
#pragma unroll
    for (int j = 0; j < 5; ++j) hv[j] = ((16 * j + l15) * 205) >> 12;
#pragma unroll
    for (int i = 0; i < 5; ++i) {
      const int hk = ((4 * i + g) * 52) >> 8;
#pragma unroll
      for (int j = 0; j < 5; ++j) {
        if (j < i - 1 || j > i + 1) continue;
        f32x4 z4 = {0.f, 0.f, 0.f, 0.f};
        f32x4 d = __builtin_amdgcn_mfma_f32_16x16x32_bf16(fr[i], fr[5 + j], z4, 0, 0, 0);
        if (hk == hv[j]) {
#pragma unroll
          for (int e = 0; e < 4; ++e) myred[320 * i + 20 * e + 16 * j] = d[e];
        }
      }
      f32x4 z4 = {0.f, 0.f, 0.f, 0.f};
      f32x4 d = __builtin_amdgcn_mfma_f32_16x16x32_bf16(fr[i], ones, z4, 0, 0, 0);
      if (l15 == 0) *(f32x4*)(red + wid * (4 * KVSZ) + 400 * (hk + 1) + 16 * i + 4 * g) = d;
    }
    __syncthreads();
    if (tid < KVSZ) {
      f32x4 a = *(const f32x4*)(red + 4 * tid);
#pragma unroll
      for (int w = 1; w < 8; ++w) a += *(const f32x4*)(red + w * (4 * KVSZ) + 4 * tid);
      *(f32x4*)(kvp + (size_t)(rb * NH + cgl * 4) * KVSZ + 4 * tid) = a;
    }
  }
}

__device__ __forceinline__ void k1_retile(const u16* __restrict__ wd, u16* __restrict__ wdt, int r, char* smem) {
  const int tid = threadIdx.x;
  const int wid = __builtin_amdgcn_readfirstlane(tid >> 6);
  const int lane = tid & 63;
  const int g = lane >> 4, l15 = lane & 15;
  char* area = smem + wid * 10240;
  unsigned boffs[5];
#pragma unroll
  for (int p = 0; p < 5; ++p) {
    int c = p * 64 + lane;
    int rho = c / 10, ch = c - rho * 10;
    int row = rho ^ (((rho >> 3) & 1) << 2);
    boffs[p] = (unsigned)(row * D_ + 8 * ch) * 2u;
  }
  const unsigned boffc = lds_addr(smem) + wid * 10240 + (8 * g + (l15 >> 2)) * 160 + (lane & 3) * 8;
  const unsigned boff0 = boffc + 640 * (g & 1), boff1 = boffc + 640 * ((g & 1) ^ 1);
#pragma unroll
  for (int hf = 0; hf < 2; ++hf) {
#pragma unroll
    for (int s = 0; s < 2; ++s) {
      const int idx = r * 8 + wid + 128 * (2 * hf + s);
      if (idx < 432) {
        const int cb = idx / 54, u = idx - cb * 54;
        const char* src = (const char*)(wd + (size_t)u * 32 * D_ + cb * 80);
#pragma unroll
        for (int p = 0; p < 5; ++p) glds16(src + boffs[p], area + s * 5120 + p * 1024);
      }
    }
    WAIT_V(0);
    sfor<2>([&](auto S) {
      constexpr int s = S.value;
      const int idx = r * 8 + wid + 128 * (2 * hf + s);
      if (idx < 432) {
        s16x4 wl[5], wh[5];
        sfor<5>([&](auto J) {
          wl[J.value] = tr_rd<s * 5120 + J.value * 32>(boff0);
          wh[J.value] = tr_rd<s * 5120 + J.value * 32>(boff1);
        });
        WAIT_L(0);
        char* dst = (char*)wdt + (size_t)idx * 5120 + lane * 16;
#pragma unroll
        for (int j = 0; j < 5; ++j) *(bf16x8*)(dst + j * 1024) = cat8(wl[j], wh[j]);
      }
    });
  }
}

__global__ __launch_bounds__(512, 2) void k1_kernel(const u16* __restrict__ x, const u16* __restrict__ wqkv,
                                                    const u16* __restrict__ wg, const u16* __restrict__ wu,
                                                    u16* __restrict__ pq, u16* __restrict__ h,
                                                    float* __restrict__ kvp, unsigned* __restrict__ flags,
                                                    const u16* __restrict__ wd, const u16* __restrict__ wo,
                                                    u16* __restrict__ wdt) {
  extern __shared__ __attribute__((aligned(16))) char smem[];
  const int bid = blockIdx.x;
  if (bid >= 240) { k1_retile(wd, wdt, bid - 240, smem); return; }
  if (bid == 0) flags[threadIdx.x] = 0u;
  const int lin = (bid & 7) * 30 + (bid >> 3);
  const int cg = lin >> 3, rb = lin & 7;
  if (cg < 18) k1_body<2>(x, wg, wu, h, kvp, rb, cg, (const char*)wo, smem);
  else if (cg < 26) k1_body<1>(x, wqkv, wqkv, pq, kvp, rb, cg - 18, (const char*)wo, smem);
  else k1_body<0>(x, wqkv, wqkv, pq, kvp, rb, cg - 26, (const char*)wo, smem);
}

constexpr int K2_REG = 20480;
constexpr int K2_LDS = 8 * K2_REG;
constexpr int K2_P_KV = 0;
constexpr int K2_P_WO = 13440;
constexpr int K2_P_KS = 18560;
constexpr int K2_S_ZL = 0;
constexpr int K2_S_ZS = 5120;
constexpr int K2_S_RD = 6144;

__device__ __forceinline__ unsigned gld32_sc1(const void* sbase, unsigned voff) {
  unsigned r; asm volatile("global_load_dword %0, %1, %2 sc1" : "=&v"(r) : "v"(voff), "s"(sbase) : "memory"); return r;
}
template <int OFF> __device__ __forceinline__ bf16x8 gld128_sc1(const void* sbase, unsigned voff) {
  bf16x8 r; asm volatile("global_load_dwordx4 %0, %1, %2 offset:%3 sc1" : "=&v"(r) : "v"(voff), "s"(sbase), "i"(OFF) : "memory"); return r;
}

__global__ __launch_bounds__(512, 2) void k2_kernel(const u16* __restrict__ x, const u16* __restrict__ h,
                                                    const u16* __restrict__ wdt, const u16* __restrict__ pq,
                                                    const float* __restrict__ kvp, const u16* __restrict__ wo,
                                                    u16* __restrict__ wp, unsigned* __restrict__ flags,
                                                    u16* __restrict__ out) {
  extern __shared__ __attribute__((aligned(16))) char smem[];
  const int bid = blockIdx.x;
  const int lin = (bid & 7) * 32 + (bid >> 3);
  const int rb = lin >> 3, cb = lin & 7;
  const int m0 = rb * 64, n0 = cb * 80;
  const int tid = threadIdx.x;
  const int wid = __builtin_amdgcn_readfirstlane(tid >> 6);
  const int lane = tid & 63;
  const int hi = wid >> 2;
  using I0 = std::integral_constant<int, 0>; using I1 = std::integral_constant<int, 1>; using I2 = std::integral_constant<int, 2>;
  const unsigned lane16 = (unsigned)lane * 16u;
  auto lid = [&]() { unsigned v = lane16; asm volatile("" : "+v"(v)); return (int)(v >> 4); };

  f32x4 acc[4][5];
  bf16x8 af[3][4], bf[3][5];
  const char* hbi[4];
#pragma unroll
  for (int i = 0; i < 4; ++i) hbi[i] = (const char*)h + (size_t)(rb * 4 + ((i + wid) & 3)) * 54 * 1024;
  const char* wtb = (const char*)wdt + (size_t)cb * 54 * 5120 + 2048;
  const int rot = (7 * cb + 14 * (rb & 3)) % 54;
  auto seof = [&](int q) { int s = q + rot; return s >= 54 ? s - 54 : s; };
  auto ldA = [&](int se, auto SL, auto I) { af[SL.value][I.value] = gld128a<0>(hbi[I.value] + (size_t)se * 1024, lane16); };
  auto ldB = [&](int se, auto SL, auto J, auto HI) {
    bf[SL.value][J.value] = gld128<((J.value + 3 * HI.value) % 5 - 2) * 1024>(wtb + (size_t)se * 5120, lane16);
  };
  auto ldU = [&](int se, auto SL, auto HI) {
    sfor<5>([&](auto J) { ldB(se, SL, J, HI); });
    sfor<4>([&](auto I) { ldA(se, SL, I); });
    __builtin_amdgcn_sched_barrier(0);
  };
  auto unit = [&](auto SL, auto HI, auto LD, int sen) {
    sfor<5>([&](auto J) {
      sfor<4>([&](auto I) {
        constexpr int i = I.value, j = J.value;
        acc[i][j] = __builtin_amdgcn_mfma_f32_16x16x32_bf16(bf[SL.value][j], af[SL.value][i], acc[i][j], 0, 0, 0);
        if constexpr (LD.value) {
          if constexpr (j == 4) ldA(sen, SL, I);
          if constexpr (i == 3) ldB(sen, SL, J, HI);
        }
        __builtin_amdgcn_sched_barrier(0);
      });
    });
  };
  auto zacc = [&]() {
#pragma unroll
    for (int i = 0; i < 4; ++i)
#pragma unroll
      for (int j = 0; j < 5; ++j) acc[i][j] = (f32x4){0.f, 0.f, 0.f, 0.f};
  };

  if (wid < 4) {
    const int ph = bid >> 3, pj = bid & 7;
    char* pm = smem + wid * K2_REG;
    if (wid < 2) {
      __builtin_amdgcn_s_setprio(3);
      const char* kb = (const char*)kvp + (size_t)(ph * KVSZ) * 4 + lane16;
#pragma unroll
      for (int r = 0; r < 8; ++r) {
        glds16(kb + (size_t)r * (NH * KVSZ * 4), pm + K2_P_KV + r * 1680);
        if (lane < 36) glds16(kb + (size_t)r * (NH * KVSZ * 4) + 1024, pm + K2_P_KV + r * 1680 + 1024);
      }
#pragma unroll
      for (int p = 0; p < 4; ++p) {
        const int c = lane + 64 * p, rho = c / 10, ch = c - rho * 10, e = rho ^ (((rho >> 3) & 1) << 2);
        if (c < 200) glds16(wo + (size_t)(ph * DH + e) * D_ + pj * 80 + ch * 8, pm + K2_P_WO + p * 1024);
      }
      __builtin_amdgcn_sched_barrier(0);
    } else {
      __builtin_amdgcn_s_setprio(1);
    }
    BAR();
    int q = wid < 2 ? 12 * wid : 15 * wid - 6;
    const int ng = wid < 2 ? 4 : 5;
    if (wid < 2) {
      const int sw = wid;
      const int lane = lid();
      const int g = lane >> 4, l15 = lane & 15;
      float* kvs = (float*)(pm + K2_P_KS);
      if (lane < 60) {
#pragma unroll
        for (int p = 0; p < 2; ++p) *(u32x4*)(pm + K2_P_WO + 3200 + (lane + 60 * p) * 16) = (u32x4){0u, 0u, 0u, 0u};
      }
      WAIT_VS(0);
      {
        const int l1 = min(lane, 35);
        f32x4 a0 = *(const f32x4*)(pm + K2_P_KV + lane * 16), a1 = *(const f32x4*)(pm + K2_P_KV + 1024 + l1 * 16);
#pragma unroll
        for (int r = 1; r < 8; ++r) {
          a0 += *(const f32x4*)(pm + K2_P_KV + r * 1680 + lane * 16);
          a1 += *(const f32x4*)(pm + K2_P_KV + r * 1680 + 1024 + l1 * 16);
        }
#pragma unroll
        for (int e = 0; e < 4; ++e) { a0[e] = rbf(a0[e]); a1[e] = rbf(a1[e]); }
        *(f32x4*)(kvs + 4 * lane) = a0;
        if (lane < 36) *(f32x4*)(kvs + 256 + 4 * lane) = a1;
      }
      WAIT_L(0);
      __builtin_amdgcn_wave_barrier();
      const unsigned wob = lds_addr(pm) + K2_P_WO + (8 * g + (l15 >> 2)) * 160 + (lane & 3) * 8 + (sw ? 96 : 0);
      const unsigned wo0 = wob + 640 * (g & 1), wo1 = wob + 640 * ((g & 1) ^ 1);
      s16x4 al[3], ah[3];
      sfor<3>([&](auto C) { al[C.value] = tr_rd<C.value * 32>(wo0); ah[C.value] = tr_rd<C.value * 32>(wo1); });
      bf16x8 yk[2];
#pragma unroll
      for (int t = 0; t < 2; ++t) {
        const int d = 16 * t + l15, dc = min(d, 19);
        f32x4 k0 = *(const f32x4*)(kvs + dc * DH + min(8 * g, 16)), k1 = *(const f32x4*)(kvs + dc * DH + min(8 * g + 4, 16));
        const bool v0 = d < DH && g < 3, v1 = d < DH && g < 2;
        u32x4 u = {v0 ? pack2(k0[0], k0[1]) : 0u, v0 ? pack2(k0[2], k0[3]) : 0u, v1 ? pack2(k1[0], k1[1]) : 0u, v1 ? pack2(k1[2], k1[3]) : 0u};
        yk[t] = __builtin_bit_cast(bf16x8, u);
      }
      WAIT_L(0);
      char* dst[2];
#pragma unroll
      for (int t = 0; t < 2; ++t) {
        const int kk = ph * DH + 16 * t + 4 * g;
        dst[t] = (char*)wp + (size_t)((pj * 20 + (kk >> 5)) * 5 + (sw ? 3 : 0)) * 1024 + (16 * ((kk & 31) >> 3) + l15) * 16 + ((kk & 7) >> 2) * 8;
      }
#pragma unroll
      for (int c = 0; c < 3; ++c) {
        const bf16x8 wa = cat8(al[c], ah[c]);
#pragma unroll
        for (int t = 0; t < 2; ++t) {
          const f32x4 z4 = {0.f, 0.f, 0.f, 0.f};
          f32x4 da = __builtin_amdgcn_mfma_f32_16x16x32_bf16(yk[t], wa, z4, 0, 0, 0);
          unsigned long long va = (unsigned long long)pack2(da[0], da[1]) | ((unsigned long long)pack2(da[2], da[3]) << 32);
          if (16 * t + 4 * g < DH && (c < 2 || !sw))
            __hip_atomic_store((unsigned long long*)(dst[t] + c * 1024), va, __ATOMIC_RELAXED, __HIP_MEMORY_SCOPE_AGENT);
        }
      }
      __builtin_amdgcn_sched_barrier(0);
      ldU(seof(q), I0{}, I0{});
      WAIT_VS(9);
      if (lane == 0) __hip_atomic_store(flags + (pj * 32 + ph) * 2 + sw, 1u, __ATOMIC_RELAXED, __HIP_MEMORY_SCOPE_AGENT);
      __builtin_amdgcn_s_setprio(1);
      __builtin_amdgcn_sched_barrier(0);
    } else {
      ldU(seof(q), I0{}, I0{});
    }
    ldU(seof(q + 1), I1{}, I0{});
    ldU(seof(q + 2), I2{}, I0{});
    zacc();
#pragma nounroll
    for (int gi = 1; gi < ng; ++gi) {
      WAIT_VS(18); unit(I0{}, I0{}, I1{}, seof(q + 3));
      WAIT_VS(18); unit(I1{}, I0{}, I1{}, seof(q + 4));
      WAIT_VS(18); unit(I2{}, I0{}, I1{}, seof(q + 5));
      q += 3;
    }
    WAIT_VS(18); unit(I0{}, I0{}, I0{}, 0);
    WAIT_VS(9); unit(I1{}, I0{}, I0{}, 0);
    WAIT_VS(0); unit(I2{}, I0{}, I0{}, 0);
  } else {
    __builtin_amdgcn_s_setprio(2);
    const int k = wid - 4;
    char* sm = smem + wid * K2_REG;
    {
      const char* zb = (const char*)kvp + (size_t)((8 * k + lane / 5) * KVSZ + DH * DH + 4 * (lane % 5)) * 4;
      if (lane < 40) {
#pragma unroll
        for (int r = 0; r < 8; ++r) glds16(zb + (size_t)r * (NH * KVSZ * 4), sm + K2_S_ZL + r * 640);
      }
      __builtin_amdgcn_sched_barrier(0);
    }
    const char* pqb[4];
#pragma unroll
    for (int i = 0; i < 4; ++i) pqb[i] = (const char*)pq + (size_t)((rb * 4 + ((i + wid) & 3)) * 20 + 5 * k) * 1024;
    bf16x8 pf[5][4];
#pragma unroll
    for (int i = 0; i < 4; ++i) pf[0][i] = gld128a<0>(pqb[i], lane16);
#pragma unroll
    for (int i = 0; i < 4; ++i) pf[1][i] = gld128a<1024>(pqb[i], lane16);
#pragma unroll
    for (int i = 0; i < 4; ++i) pf[2][i] = gld128a<2048>(pqb[i], lane16);
#pragma unroll
    for (int i = 0; i < 4; ++i) pf[3][i] = gld128a<3072>(pqb[i], lane16);
#pragma unroll
    for (int i = 0; i < 4; ++i) pf[4][i] = gld128a<0>(pqb[i] + 4096, lane16);
    __builtin_amdgcn_sched_barrier(0);
    BAR();
    float* zs = (float*)(sm + K2_S_ZS);
    float* rden = (float*)(sm + K2_S_RD);
    WAIT_VS(0);
#pragma unroll
    for (int u = 0; u < 5; ++u)
#pragma unroll
      for (int i = 0; i < 4; ++i) asm volatile("" : "+a"(pf[u][i]));
    __builtin_amdgcn_sched_barrier(0);
    {
      const int lane = lid();
      const int l1 = min(lane, 39);
      f32x4 z0 = *(const f32x4*)(sm + K2_S_ZL + l1 * 16);
#pragma unroll
      for (int r = 1; r < 8; ++r) z0 += *(const f32x4*)(sm + K2_S_ZL + r * 640 + l1 * 16);
#pragma unroll
      for (int e = 0; e < 4; ++e) z0[e] = rbf(z0[e]);
      *(f32x4*)(zs + 4 * lane) = z0;
    }
    WAIT_L(0);
    __builtin_amdgcn_wave_barrier();
    {
      const int lq = lid();
      const int g = lq >> 4, l15 = lq & 15;
      f32x4 dn[4];
#pragma unroll
      for (int i = 0; i < 4; ++i) dn[i] = (f32x4){0.f, 0.f, 0.f, 0.f};
#pragma unroll
      for (int u = 0; u < 5; ++u) {
        f32x4 z0 = *(const f32x4*)(zs + 32 * u + 8 * g), z1 = *(const f32x4*)(zs + 32 * u + 8 * g + 4);
        float zv[8] = {z0[0], z0[1], z0[2], z0[3], z1[0], z1[1], z1[2], z1[3]};
        const int k0 = 32 * u + 8 * g;
        const int h0 = k0 / DH, lim = (h0 + 1) * DH - k0;
        const bool m0_ = (h0 == l15), m1_ = (h0 + 1 == l15);
        u32x4 zm;
#pragma unroll
        for (int e = 0; e < 4; ++e) {
          bool m = (2 * e < lim) ? m0_ : m1_;
          zm[e] = m ? pack2(zv[2 * e], zv[2 * e + 1]) : 0u;
        }
        bf16x8 zz = __builtin_bit_cast(bf16x8, zm);
#pragma unroll
        for (int i = 0; i < 4; ++i) dn[i] = __builtin_amdgcn_mfma_f32_16x16x32_bf16(zz, pf[u][i], dn[i], 0, 0, 0);
        __builtin_amdgcn_sched_barrier(0);
      }
      if (g < 2) {
#pragma unroll
        for (int i = 0; i < 4; ++i) {
          f32x4 r;
#pragma unroll
          for (int e = 0; e < 4; ++e) r[e] = __builtin_amdgcn_rcpf(rbf(rbf(dn[i][e]) + 1e-6f));
          *(f32x4*)(rden + (i * 16 + l15) * 8 + 4 * g) = r;
        }
      }
      __builtin_amdgcn_s_waitcnt(0xc07f);
      __builtin_amdgcn_wave_barrier();
#pragma unroll
      for (int u = 0; u < 5; ++u) {
        const int k0 = 32 * u + 8 * g;
        const int h0 = k0 / DH;
        const int lim = (h0 + 1) * DH - k0;
#pragma unroll
        for (int i = 0; i < 4; ++i) {
          const float* rp = rden + (i * 16 + l15) * 8 + h0;
          float r0 = rp[0], r1 = rp[1];
          u32x4 uu = __builtin_bit_cast(u32x4, pf[u][i]);
          u32x4 o;
#pragma unroll
          for (int e = 0; e < 4; ++e) {
            float r = (2 * e < lim) ? r0 : r1;
            o[e] = pack2(bflo(uu[e]) * r, bfhi(uu[e]) * r);
          }
          pf[u][i] = __builtin_bit_cast(bf16x8, o);
        }
        __builtin_amdgcn_sched_barrier(0);
      }
      __builtin_amdgcn_s_waitcnt(0xc07f);
      __builtin_amdgcn_sched_barrier(0);
    }
    __builtin_amdgcn_s_setprio(3);
    {
      const unsigned* fp = flags + (cb * 32 + 8 * k) * 2;
      const unsigned fo = (lane16 >> 2) & 60u;
      __builtin_amdgcn_s_sleep(40);
      for (;;) {
        unsigned f = gld32_sc1(fp, fo);
        WAIT_VS(0);
        asm volatile("" : "+v"(f));
        if (__all(f == 1u)) break;
        __builtin_amdgcn_s_sleep(8);
      }
    }
    __builtin_amdgcn_sched_barrier(0);
    const char* wpb = (const char*)wp + (size_t)(cb * 20 + 5 * k) * 5120 + 2048;
    auto ldS = [&](auto U) {
      sfor<5>([&](auto J) {
        bf[U.value % 3][J.value] = gld128_sc1<((J.value + 3) % 5 - 2) * 1024>(wpb + U.value * 5120, lane16);
      });
      __builtin_amdgcn_sched_barrier(0);
    };
    ldS(I0{}); ldS(I1{}); ldS(I2{});
    zacc();
    __builtin_amdgcn_sched_barrier(0);
    sfor<5>([&](auto UU) {
      constexpr int u = UU.value;
      if constexpr (u <= 2) WAIT_VS(10); else if constexpr (u == 3) WAIT_VS(5); else WAIT_VS(0);
      sfor<5>([&](auto J) {
        sfor<4>([&](auto I) {
          constexpr int i = I.value, j = J.value;
          acc[i][j] = __builtin_amdgcn_mfma_f32_16x16x32_bf16(bf[u % 3][j], pf[u][i], acc[i][j], 0, 0, 0);
          __builtin_amdgcn_sched_barrier(0);
        });
      });
      if constexpr (u + 3 < 5) ldS(std::integral_constant<int, u + 3>{});
    });
  }

  int ln;
  asm volatile("v_mbcnt_lo_u32_b32 %0, -1, 0\n\tv_mbcnt_hi_u32_b32 %0, -1, %0" : "=v"(ln));
  const int g2 = ln >> 4;
  const unsigned orow = (unsigned)(m0 + (wid & 3) * 16 + (ln & 15));
  u32x2 xv[3];
#pragma unroll
  for (int j = 0; j < 3; ++j) xv[j] = *(const u32x2*)(x + (size_t)orow * D_ + n0 + ((j + 3 * hi) % 5) * 16 + 4 * g2);
  {
    char* myr = smem + wid * K2_REG + ln * 16;
#pragma unroll
    for (int i = 0; i < 4; ++i)
#pragma unroll
      for (int j = 0; j < 5; ++j) *(f32x4*)(myr + (((i + wid) & 3) * 5 + (j + 3 * hi) % 5) * 1024) = acc[i][j];
  }
  __syncthreads();
  f32x2 ys[6], ss[6];
#pragma unroll
  for (int n = 0; n < 6; ++n) { ys[n] = (f32x2){0.f, 0.f}; ss[n] = (f32x2){0.f, 0.f}; }
#pragma unroll
  for (int w = 0; w < 8; ++w) {
    const char* src = smem + w * K2_REG + ln * 16 + (wid & 3) * 5 * 1024;
#pragma unroll
    for (int j = 0; j < 3; ++j) {
      if (j == 2 && hi) continue;
      f32x4 a = *(const f32x4*)(src + ((j + 3 * hi) % 5) * 1024);
      const f32x2 a0 = {a[0], a[1]}, a1 = {a[2], a[3]};
      if (w == 0) { ys[2 * j] = a0; ys[2 * j + 1] = a1; }
      else if (w < 4) { ys[2 * j] += a0; ys[2 * j + 1] += a1; }
      else if (w == 4) { ss[2 * j] = a0; ss[2 * j + 1] = a1; }
      else { ss[2 * j] += a0; ss[2 * j + 1] += a1; }
    }
  }
  auto fin = [&](auto N0, auto NN) {
    constexpr int n0_ = N0.value, N = NN.value;
    f32x2 y[N], sv[N], t[N];
#pragma unroll
    for (int n = 0; n < N; ++n) { y[n] = ys[n0_ + n]; sv[n] = ss[n0_ + n]; }
    rbf4(y);
#pragma unroll
    for (int n = 0; n < N; ++n) t[n] = y[n] * -LOG2E;
#pragma unroll
    for (int n = 0; n < N; ++n) t[n] = exp2_2(t[n]);
    rbf4(sv);
#pragma unroll
    for (int n = 0; n < N; ++n) t[n] = t[n] + 1.0f;
#pragma unroll
    for (int n = 0; n < N; ++n) t[n] = (f32x2){__builtin_amdgcn_rcpf(t[n][0]), __builtin_amdgcn_rcpf(t[n][1])};
#pragma unroll
    for (int n = 0; n < N; ++n) t[n] = y[n] * t[n];
    rbf4(t);
#pragma unroll
    for (int n = 0; n < N; ++n) t[n] = sv[n] * t[n];
    rbf4(t);
#pragma unroll
    for (int jj = 0; jj < N / 2; ++jj) {
      const int j = n0_ / 2 + jj;
      const size_t o = (size_t)orow * D_ + n0 + ((j + 3 * hi) % 5) * 16 + 4 * g2;
      u32x2 ov = {pk2(up2(xv[j][0]) + t[2 * jj]), pk2(up2(xv[j][1]) + t[2 * jj + 1])};
      *(u32x2*)(out + o) = ov;
    }
  };
  fin(std::integral_constant<int, 0>{}, std::integral_constant<int, 4>{});
  if (!hi) fin(std::integral_constant<int, 4>{}, std::integral_constant<int, 2>{});
}

extern "C" void kernel_launch(void* const* d_in, const int* in_sizes, int n_in, void* d_out, int out_size,
                              void* d_ws, size_t ws_size, hipStream_t stream) {
  const u16* x = (const u16*)d_in[0];
  const u16* wqkv = (const u16*)d_in[1];
  const u16* wo = (const u16*)d_in[2];
  const u16* wg = (const u16*)d_in[4];
  const u16* wu = (const u16*)d_in[5];
  const u16* wd = (const u16*)d_in[6];
  char* ws = (char*)d_ws;
  u16* pq = (u16*)(ws);
  u16* h = (u16*)(ws + (4 << 20));
  float* kvp = (float*)(ws + (12 << 20));
  u16* wp = (u16*)(ws + (13 << 20));
  unsigned* flags = (unsigned*)(ws + (14 << 20));
  u16* wdt = (u16*)(ws + (16 << 20));
  (void)hipFuncSetAttribute((const void*)k1_kernel, hipFuncAttributeMaxDynamicSharedMemorySize, K1_LDS);
  (void)hipFuncSetAttribute((const void*)k2_kernel, hipFuncAttributeMaxDynamicSharedMemorySize, K2_LDS);
  k1_kernel<<<256, 512, K1_LDS, stream>>>(x, wqkv, wg, wu, pq, h, kvp, flags, wd, wo, wdt);
  k2_kernel<<<256, 512, K2_LDS, stream>>>(x, h, wdt, pq, kvp, wo, wp, flags, (u16*)d_out);
}
```

```cpp
#include <hip/hip_runtime.h>
#include <hip/hip_bf16.h>
#include <stdint.h>
#include <utility>

typedef unsigned short u16;
typedef __attribute__((ext_vector_type(8))) short bf16x8;
typedef __attribute__((ext_vector_type(4))) short s16x4;
typedef __attribute__((ext_vector_type(4))) float f32x4;
typedef __attribute__((ext_vector_type(2))) __bf16 bf2;
typedef __attribute__((ext_vector_type(4))) unsigned u32x4;
typedef __attribute__((ext_vector_type(2))) unsigned u32x2;
#define LAS __attribute__((address_space(3)))

constexpr int S_ = 2048, D_ = 640, F_ = 1728, NH = 32, DH = 20;
constexpr int KVSZ = DH * DH + DH;

__device__ __forceinline__ unsigned pack2(float a, float b) {
  bf2 v = {(__bf16)a, (__bf16)b};
  return __builtin_bit_cast(unsigned, v);
}
__device__ __forceinline__ float rbf(float a) { return (float)(__bf16)a; }
__device__ __forceinline__ float bflo(unsigned u) { return __uint_as_float(u << 16); }
__device__ __forceinline__ float bfhi(unsigned u) { return __uint_as_float(u & 0xffff0000u); }
__device__ __forceinline__ float dot_ones(unsigned a, float c) {
  bf2 one = {(__bf16)1.0f, (__bf16)1.0f};
  return __builtin_amdgcn_fdot2_f32_bf16(__builtin_bit_cast(bf2, a), one, c, false);
}
__device__ __forceinline__ float dot_self(unsigned a, float c) {
  bf2 v = __builtin_bit_cast(bf2, a);
  return __builtin_amdgcn_fdot2_f32_bf16(v, v, c, false);
}
typedef __attribute__((ext_vector_type(2))) float f32x2;
__device__ __forceinline__ f32x2 up2(unsigned p) { return (f32x2){bflo(p), bfhi(p)}; }
__device__ __forceinline__ unsigned pk2(f32x2 v) { return pack2(v[0], v[1]); }
__device__ __forceinline__ f32x2 rbf2(f32x2 v) {
  unsigned p = pk2(v); asm("" : "+v"(p)); return up2(p);
}
__device__ __forceinline__ f32x2 exp2_2(f32x2 a) { return (f32x2){__builtin_amdgcn_exp2f(a[0]), __builtin_amdgcn_exp2f(a[1])}; }
constexpr float LOG2E = 1.44269504088896340736f;
__device__ __forceinline__ void rbf4(f32x2 (&v)[4]) {
  unsigned p[4];
#pragma unroll
  for (int n = 0; n < 4; ++n) p[n] = pk2(v[n]);
  asm("" : "+v"(p[0]), "+v"(p[1]), "+v"(p[2]), "+v"(p[3]));
#pragma unroll
  for (int n = 0; n < 4; ++n) v[n] = up2(p[n]);
}
__device__ __forceinline__ void rbf4(f32x2 (&v)[2]) {
  unsigned p0 = pk2(v[0]), p1 = pk2(v[1]);
  asm("" : "+v"(p0), "+v"(p1));
  v[0] = up2(p0); v[1] = up2(p1);
}
__device__ __forceinline__ float max0(float x) { float r; asm("v_max_f32 %0, 0, %1" : "=v"(r) : "v"(x)); return r; }
__device__ __forceinline__ void phi4(f32x2 (&t)[4], unsigned (&o)[4]) {
  rbf4(t);
  f32x2 ex[4], p[4];
#pragma unroll
  for (int n = 0; n < 4; ++n) ex[n] = t[n] * LOG2E;
#pragma unroll
  for (int n = 0; n < 4; ++n) {
    ex[n] = exp2_2(ex[n]);
    ex[n] = (f32x2){__builtin_amdgcn_fmed3f(ex[n][0], 0.f, 1.f), __builtin_amdgcn_fmed3f(ex[n][1], 0.f, 1.f)};
  }
#pragma unroll
  for (int n = 0; n < 4; ++n) p[n] = (f32x2){max0(t[n][0]), max0(t[n][1])};
#pragma unroll
  for (int n = 0; n < 4; ++n) p[n] = p[n] + ex[n];
  rbf4(p);
#pragma unroll
  for (int n = 0; n < 4; ++n) p[n] = p[n] + 1e-6f;
#pragma unroll
  for (int n = 0; n < 4; ++n) o[n] = pk2(p[n]);
}
__device__ __forceinline__ void glds16(const void* g, void* l) {
  __builtin_amdgcn_global_load_lds((const unsigned*)g, (unsigned*)l, 16, 0, 0);
}
template <class F, int... I> __device__ __forceinline__ void sfor_impl(F&& f, std::integer_sequence<int, I...>) {
  (f(std::integral_constant<int, I>{}), ...);
}
template <int N, class F> __device__ __forceinline__ void sfor(F&& f) { sfor_impl(f, std::make_integer_sequence<int, N>{}); }
#define WAIT_V(n) asm volatile("s_waitcnt vmcnt(" #n ")" ::: "memory")
#define WAIT_VS(n) do { WAIT_V(n); __builtin_amdgcn_sched_barrier(0); } while (0)
#define BAR() __builtin_amdgcn_s_barrier()
template <int OFF> __device__ __forceinline__ s16x4 tr_rd(unsigned a) {
  s16x4 r; asm volatile("ds_read_b64_tr_b16 %0, %1 offset:%2" : "=&v"(r) : "v"(a), "i"(OFF) : "memory"); return r;
}
template <int OFF> __device__ __forceinline__ bf16x8 lds_rd128(unsigned a) {
  bf16x8 r; asm volatile("ds_read_b128 %0, %1 offset:%2" : "=&v"(r) : "v"(a), "i"(OFF) : "memory"); return r;
}
template <int OFF> __device__ __forceinline__ bf16x8 gld128(const void* sbase, unsigned voff) {
  bf16x8 r; asm volatile("global_load_dwordx4 %0, %1, %2 offset:%3" : "=&v"(r) : "v"(voff), "s"(sbase), "i"(OFF) : "memory"); return r;
}
template <int OFF> __device__ __forceinline__ bf16x8 gld128a(const void* sbase, unsigned voff) {
  bf16x8 r; asm volatile("global_load_dwordx4 %0, %1, %2 offset:%3" : "=&a"(r) : "v"(voff), "s"(sbase), "i"(OFF) : "memory"); return r;
}
__device__ __forceinline__ bf16x8 cat8(s16x4 a, s16x4 b) { return __builtin_shufflevector(a, b, 0, 1, 2, 3, 4, 5, 6, 7); }
#define WAIT_L(n) do { asm volatile("s_waitcnt lgkmcnt(" #n ")" ::: "memory"); __builtin_amdgcn_sched_barrier(0); } while (0)
__device__ __forceinline__ unsigned lds_addr(const void* p) { return (unsigned)(size_t)(LAS const char*)p; }
__device__ __forceinline__ float phi_bf(float t) {
  float p = t > 0.f ? rbf(t + 1.0f) : rbf(__expf(t));
  return rbf(p + 1e-6f);
}

constexpr int K1_ABYTES = 256 * 64 * 2;
constexpr int K1_BBYTES = 64 * 192 * 2;
constexpr int K1_OFF_B = 3 * K1_ABYTES;
constexpr int K1_OFF_MISC = K1_OFF_B + 2 * K1_BBYTES;
constexpr int K1_OFF_DUMMY = K1_OFF_MISC;
constexpr int K1_OFF_MEAN = K1_OFF_DUMMY + 1024;
constexpr int K1_OFF_CSF = K1_OFF_MEAN + 2048;
constexpr int K1_LDS = K1_OFF_CSF + 768;
constexpr int K1_OFF_RS = K1_ABYTES;
constexpr int K1_OFF_CS = 2 * K1_ABYTES;

template <int ROLE>
__device__ __forceinline__ void k1_body(const u16* __restrict__ x, const u16* __restrict__ w0,
                                        const u16* __restrict__ w1, u16* __restrict__ outp,
                                        float* __restrict__ kvp, int rb, int cgl,
                                        const char* __restrict__ wop, char* smem) {
  constexpr int BN = ROLE == 2 ? 192 : 160;
  constexpr int NB = BN / 16;
  constexpr int WM = ROLE == 1 ? 8 : 4;
  constexpr int WN = 8 / WM;
  constexpr int MT = 16 / WM;
  constexpr int NT = NB / WN;
  constexpr int BPIECES = 64 * BN * 2 / 1024;
  constexpr int LDW = ROLE == 2 ? F_ : 3 * D_;
  constexpr int RBI = ROLE == 2 ? 192 : ROLE == 0 ? 320 : 160;
  constexpr int CPR = RBI / 16;
  constexpr int PPI = 64 * RBI / 1024;
  constexpr int IMGB = 64 * RBI;

  const int tid = threadIdx.x;
  const int wid = __builtin_amdgcn_readfirstlane(tid >> 6);
  const int lane = tid & 63;
  const int g = lane >> 4, l15 = lane & 15;
  const int wm = wid % WM, wn = wid / WM;
  const int m0 = rb * 256;
  const int CSOFF = 2 * wn;

  const char* abase = (const char*)(x + (size_t)m0 * D_);
  const unsigned aoffg = (unsigned)(((wid * 8 + (lane >> 3)) * D_ + (((lane & 7) ^ (lane >> 3)) * 8)) * 2);
  const char* bptr[3];
#pragma unroll
  for (int i = 0; i < 3; ++i) {
    int p = wid + 8 * i;
    if (p >= BPIECES) p = wid;
    const int img = p / PPI, c = (p - img * PPI) * 64 + lane;
    const int rho = c / CPR, chp = c - rho * CPR;
    const int k = ROLE == 1 ? (rho ^ (((rho >> 3) & 1) << 2)) : rho;
    const int ch = ROLE == 1 ? chp : (chp ^ (2 * ((rho >> 3) & 1)));
    const u16* wp;
    if (ROLE == 0) wp = w0 + cgl * 160;
    else if (ROLE == 1) wp = w0 + (img ? 2 * D_ : D_) + cgl * 80;
    else wp = (img ? w1 : w0) + cgl * 96;
    bptr[i] = (const char*)(wp + (size_t)k * LDW + ch * 8);
  }
  auto loadA = [&](int t, int slot, int i) {
    glds16(abase + (size_t)(i * 64 * D_ * 2 + t * 128) + aoffg, smem + slot * K1_ABYTES + (wid + 8 * i) * 1024);
  };
  auto loadB = [&](int t, int slot, int i) {
    if (wid + 8 * i < BPIECES) {
      glds16(bptr[i] + (size_t)t * (64 * LDW * 2), smem + K1_OFF_B + slot * K1_BBYTES + (wid + 8 * i) * 1024);
    }
  };
  auto stageA = [&](int t, int slot) {
#pragma unroll
    for (int i = 0; i < 4; ++i) loadA(t, slot, i);
  };
  auto stageB = [&](int t, int slot) {
#pragma unroll
    for (int i = 0; i < 3; ++i) loadB(t, slot, i);
  };

  constexpr int SEG = ROLE == 2 ? 192 : ROLE == 0 ? 320 : 160, NIMG = ROLE == 0 ? 1 : 2, NQ = (SEG + 127) / 128 + 1;
  const char* pfb;
  {
    const int li = lane % (NIMG * NQ), img = li / NQ, q = li - img * NQ;
    const u16* wp;
    if (ROLE == 0) wp = w0 + cgl * 160;
    else if (ROLE == 1) wp = w0 + (img ? 2 * D_ : D_) + cgl * 80;
    else wp = (img ? w1 : w0) + cgl * 96;
    pfb = (const char*)(wp + (size_t)(rb * 8 + wid) * LDW) + min(q * 128, SEG - 4);
  }
  auto pfB = [&](int t) {
    __builtin_amdgcn_global_load_lds((const unsigned*)(pfb + (size_t)t * (64 * LDW * 2)), (unsigned*)(smem + K1_OFF_DUMMY), 4, 0, 0);
  };
  const char* pfo = wop + (size_t)((blockIdx.x * 8 + wid) % 800) * 1024 + lane * 16;
  stageA(0, 0);
  stageB(0, 0);
  pfB(2);
  stageA(1, 1);

  const int arow = wm * (MT * 16) + l15;
  const unsigned lbase = lds_addr(smem);
  const unsigned aoff0 = lbase + arow * 128 + ((g ^ (lane & 7)) * 16);
  const int bq = l15 >> 2, bp = lane & 3;
  constexpr int NBJ = ROLE == 2 ? 3 : ROLE == 0 ? 5 : 2;
  unsigned bj[NBJ];
  if (ROLE == 1) {
    bj[0] = lbase + K1_OFF_B + (8 * g + bq + 4 * (g & 1)) * RBI + bp * 8;
    bj[1] = lbase + K1_OFF_B + (8 * g + bq + 4 * ((g & 1) ^ 1)) * RBI + bp * 8;
  } else {
#pragma unroll
    for (int j = 0; j < NBJ; ++j) bj[j] = lbase + K1_OFF_B + (8 * g + bq) * RBI + (((wn * NBJ + j) ^ (g & 1)) * 32) + bp * 8;
  }

  f32x4 acc[MT][NT];
#pragma unroll
  for (int i = 0; i < MT; ++i)
#pragma unroll
    for (int j = 0; j < NT; ++j) acc[i][j] = (f32x4){0.f, 0.f, 0.f, 0.f};
  float rs[MT], rq[MT], cs[NT];
#pragma unroll
  for (int i = 0; i < MT; ++i) { rs[i] = 0.f; rq[i] = 0.f; }
#pragma unroll
  for (int j = 0; j < NT; ++j) cs[j] = 0.f;

  bf16x8 xf[2][MT];
  s16x4 wl[2][NT], wh[2][NT];
  auto rd = [&](auto S, unsigned soa, unsigned sob) {
    constexpr int s = S.value;
    const unsigned aa = (aoff0 ^ (s * 64)) + soa;
    sfor<MT>([&](auto I) { xf[s][I.value] = lds_rd128<I.value * 2048>((I.value & 1) ? aa - wn * 2048 : aa + wn * 2048); });
    unsigned bs[NBJ];
#pragma unroll
    for (int j = 0; j < NBJ; ++j) bs[j] = bj[j] + sob;
    sfor<NT>([&](auto J) {
      constexpr int j = J.value;
      if constexpr (ROLE == 1) {
        constexpr int off = (j / 5) * IMGB + s * 32 * RBI + (j % 5) * 32;
        wl[s][j] = tr_rd<off>(bs[0]);
        wh[s][j] = tr_rd<off>(bs[1]);
      } else {
        constexpr int off = (j / NBJ) * IMGB + s * 32 * RBI;
        wl[s][j] = tr_rd<off>(bs[j % NBJ]);
        wh[s][j] = tr_rd<off + 4 * RBI>(bs[j % NBJ]);
      }
    });
  };
  auto rd1 = [&](auto S, auto R, unsigned aa, const unsigned* bs) {
    constexpr int s = S.value, r = R.value;
    if constexpr (r < MT) {
      xf[s][r] = lds_rd128<r * 2048>((r & 1) ? aa - wn * 2048 : aa + wn * 2048);
    } else {
      constexpr int j = (r - MT) / 2, hi = (r - MT) & 1;
      if constexpr (ROLE == 1) {
        constexpr int off = (j / 5) * IMGB + s * 32 * RBI + (j % 5) * 32;
        if constexpr (hi) wh[s][j] = tr_rd<off>(bs[1]); else wl[s][j] = tr_rd<off>(bs[0]);
      } else {
        constexpr int off = (j / NBJ) * IMGB + s * 32 * RBI;
        if constexpr (hi) wh[s][j] = tr_rd<off + 4 * RBI>(bs[j % NBJ]); else wl[s][j] = tr_rd<off>(bs[j % NBJ]);
      }
    }
  };
  auto mm = [&](auto S, auto LDX, int t, int nslot, bool do_cs, auto RSX, unsigned soa, unsigned sob) {
    constexpr int s = S.value, LD = LDX.value, RS = RSX.value;
    constexpr int NRDX = MT + 2 * NT;
    const unsigned aa = (aoff0 ^ ((RS & 1) * 64)) + soa;
    unsigned bs[NBJ];
#pragma unroll
    for (int j = 0; j < NBJ; ++j) bs[j] = bj[j] + sob;
    bf16x8 wf[NT];
#pragma unroll
    for (int j = 0; j < NT; ++j) wf[j] = cat8(wl[s][j], wh[s][j]);
    constexpr int NM = MT * NT;
    constexpr int ND = (WN == 2) ? (MT / 2) * 8 : MT * 8;
    sfor<NM>([&](auto X) {
      constexpr int xi = X.value, i = xi / NT, j = xi % NT;
      if (ROLE == 1) acc[i][j] = __builtin_amdgcn_mfma_f32_16x16x32_bf16(xf[s][i], wf[j], acc[i][j], 0, 0, 0);
      else acc[i][j] = __builtin_amdgcn_mfma_f32_16x16x32_bf16(wf[j], xf[s][i], acc[i][j], 0, 0, 0);
      constexpr int d0 = xi * ND / NM, d1 = (xi + 1) * ND / NM;
      sfor<d1 - d0>([&](auto Y) {
        constexpr int d = d0 + Y.value;
        constexpr int NTL = ND / 8;
        constexpr int q = (d >> 1) % NTL, e = (d >> 1) / NTL;
        constexpr int ii = (WN == 2) ? 2 * q : q;
        unsigned u = __builtin_bit_cast(u32x4, xf[s][ii])[e];
        if (d & 1) rq[q] = dot_self(u, rq[q]); else rs[q] = dot_ones(u, rs[q]);
      });
      if constexpr (RS < 2) {
        constexpr int NMR = (NM * 3) / 4;
        constexpr int r0 = xi < NMR ? xi * NRDX / NMR : NRDX, r1 = xi + 1 < NMR ? (xi + 1) * NRDX / NMR : NRDX;
        sfor<r1 - r0>([&](auto Y) { rd1(std::integral_constant<int, RS & 1>{}, std::integral_constant<int, r0 + Y.value>{}, aa, bs); });
      }
      constexpr int P = NM / 4;
      if constexpr (LD == 0 && (xi == 1 || xi == 1 + P || xi == 1 + 2 * P)) { if (t + 1 < 10) loadB(t + 1, (t + 1) & 1, (xi - 1) / P); else if (xi == 1) glds16(pfo, smem + K1_OFF_DUMMY); }
      if constexpr (LD == 0 && xi == 1 + 3 * P) { if (t + 2 < 10) loadA(t + 2, nslot, 0); }
      if constexpr (LD == 1 && (xi == 1 || xi == 1 + P || xi == 1 + 2 * P)) {
        if (t + 2 < 10) loadA(t + 2, nslot, 1 + (xi - 1) / P);
      }
      if constexpr (LD == 1 && xi == 1 + 3 * P) { if (t + 2 < 10) pfB(t + 3 < 10 ? t + 3 : 9); }
      __builtin_amdgcn_sched_barrier(0);
    });
    if (do_cs) {
#pragma unroll
      for (int j = 0; j < NT; ++j) {
        u32x4 u = __builtin_bit_cast(u32x4, wf[j]);
#pragma unroll
        for (int e = 0; e < 4; ++e) cs[j] = dot_ones(u[e], cs[j]);
      }
    }
  };
  using I0 = std::integral_constant<int, 0>; using I1 = std::integral_constant<int, 1>; using I2 = std::integral_constant<int, 2>;
  constexpr int NRD = MT + 2 * NT;

  int aslot = 0;
  bool last_cs;
  {
    bool prev_cs = (CSOFF % WM) == wm;
    {
      WAIT_V(4);
      BAR();
      rd(I0{}, 0u, 0u);
      stageB(1, 1);
      loadA(2, 2, 0);
      WAIT_L(0);
      mm(I0{}, I1{}, 0, 2, prev_cs, I1{}, 0u, 0u);
      WAIT_L(0);
      aslot = 1;
    }
#pragma nounroll
    for (int t = 1; t < 10; ++t) {
      if (t < 9) WAIT_V(5); else WAIT_V(0);
      BAR();
      const int nslot = aslot == 0 ? 2 : aslot - 1;
      const unsigned soa = aslot * K1_ABYTES, sob = (t & 1) * K1_BBYTES;
      aslot = aslot == 2 ? 0 : aslot + 1;
      const bool do_cs = ((t + CSOFF) % WM) == wm;
      mm(I1{}, I0{}, t, nslot, prev_cs, I0{}, soa, sob);
      WAIT_L(0);
      mm(I0{}, I1{}, t, nslot, do_cs, I1{}, soa, sob);
      WAIT_L(0);
      prev_cs = do_cs;
    }
    last_cs = prev_cs;
  }

  float* rsum = (float*)(smem + K1_OFF_RS);
  float* csum = (float*)(smem + K1_OFF_CS);
  float* smean = (float*)(smem + K1_OFF_MEAN);
  float* srsd = smean + 256;
  float* scs = (float*)(smem + K1_OFF_CSF);
  {
    bf16x8 wf[NT];
#pragma unroll
    for (int j = 0; j < NT; ++j) wf[j] = cat8(wl[1][j], wh[1][j]);
#pragma unroll
    for (int e = 0; e < 4; ++e)
#pragma unroll
      for (int q = 0; q < (WN == 2 ? MT / 2 : MT); ++q) {
        const unsigned u = __builtin_bit_cast(u32x4, xf[1][(WN == 2) ? 2 * q : q])[e];
        rs[q] = dot_ones(u, rs[q]);
        rq[q] = dot_self(u, rq[q]);
      }
    if (last_cs) {
#pragma unroll
      for (int e = 0; e < 4; ++e)
#pragma unroll
        for (int j = 0; j < NT; ++j) cs[j] = dot_ones(__builtin_bit_cast(u32x4, wf[j])[e], cs[j]);
    }
#pragma unroll
    for (int q = 0; q < (WN == 2 ? MT / 2 : MT); ++q) {
      int r = wm * (MT * 16) + ((WN == 2) ? ((2 * q) ^ wn) : q) * 16 + l15;
      float2 v = {rs[q], rq[q]};
      *(float2*)(rsum + (g * 256 + r) * 2) = v;
    }
#pragma unroll
    for (int j = 0; j < NT; ++j) {
      int nb = (ROLE == 2) ? ((j < 3) ? (wn * 3 + j) : (6 + wn * 3 + (j - 3))) : (wn * NT + j);
      csum[(wm * 4 + g) * 192 + nb * 16 + l15] = cs[j];
    }
    WAIT_L(0);
    BAR();
    constexpr int NP = WM * 4;
    float pr[NP];
    const int cn = (tid - 256 < BN) ? tid - 256 : 0;
    if (wid < 4) {
#pragma unroll
      for (int w = 0; w < 4; ++w) { float2 v = *(const float2*)(rsum + (w * 256 + tid) * 2); pr[2 * w] = v.x; pr[2 * w + 1] = v.y; }
#pragma unroll
      for (int w = 8; w < NP; ++w) pr[w] = 0.f;
    } else {
#pragma unroll
      for (int w = 0; w < NP; ++w) pr[w] = csum[w * 192 + cn];
    }
    __builtin_amdgcn_sched_barrier(0);
    constexpr int NM = MT * NT;
    auto mf = [&](auto LO, auto HI) {
      sfor<HI.value - LO.value>([&](auto X) {
        constexpr int xi = LO.value + X.value, i = xi / NT, j = xi % NT;
        if (ROLE == 1) acc[i][j] = __builtin_amdgcn_mfma_f32_16x16x32_bf16(xf[1][i], wf[j], acc[i][j], 0, 0, 0);
        else acc[i][j] = __builtin_amdgcn_mfma_f32_16x16x32_bf16(wf[j], xf[1][i], acc[i][j], 0, 0, 0);
      });
      __builtin_amdgcn_sched_barrier(0);
    };
    mf(I0{}, std::integral_constant<int, NM / 3>{});
    if (wid < 4) {
      float a = 0.f, b = 0.f;
#pragma unroll
      for (int w = 0; w < 4; ++w) { a += pr[2 * w]; b += pr[2 * w + 1]; }
      float mean = a * (1.0f / D_);
      float var = fmaxf(b * (1.0f / D_) - mean * mean, 0.f);
      float sd = rbf(sqrtf(rbf(rbf(var) + 1e-5f)));
      smean[tid] = rbf(mean);
      srsd[tid] = 1.0f / sd;
    } else {
      float a = 0.f;
#pragma unroll
      for (int w = 0; w < NP; ++w) a += pr[w];
      if (tid - 256 < BN) scs[cn] = a;
    }
    __builtin_amdgcn_sched_barrier(0);
    mf(std::integral_constant<int, NM / 3>{}, std::integral_constant<int, NM>{});
  }
  WAIT_L(0);
  BAR();

  if (ROLE == 0) {
    float mean[MT], rsd[MT];
    f32x4 c4[NT];
#pragma unroll
    for (int i = 0; i < MT; ++i) { int r = wm * 64 + (i ^ wn) * 16 + l15; mean[i] = smean[r]; rsd[i] = srsd[r]; }
#pragma unroll
    for (int j = 0; j < NT; ++j) c4[j] = *(const f32x4*)(scs + (wn * NT + j) * 16 + 4 * g);
    char* wb = (char*)outp + (size_t)((rb * 16 + wm * 4) * 20 + cgl * 5) * 1024 +
               (unsigned)(((g & 1) ^ wn) * 20 * 1024 + wn * 5 * 512 + (g >> 1) * 256 + l15 * 16);
    __builtin_amdgcn_sched_barrier(0);
#pragma unroll
    for (int ip = 0; ip < MT; ip += 2) {
#pragma unroll
      for (int j = 0; j < NT; ++j) {
        f32x2 t[4];
        unsigned o[4];
#pragma unroll
        for (int n = 0; n < 4; ++n) {
          const int i = ip + (n >> 1), e = 2 * (n & 1);
          t[n] = ((f32x2){acc[i][j][e], acc[i][j][e + 1]} - mean[i] * (f32x2){c4[j][e], c4[j][e + 1]}) * rsd[i];
        }
        phi4(t, o);
        const auto s0 = __builtin_amdgcn_permlane16_swap(o[0], o[2], false, false);
        const auto s1 = __builtin_amdgcn_permlane16_swap(o[1], o[3], false, false);
        *(u32x4*)(wb + ip * 20 * 1024 + j * 512) = (u32x4){s0[0], s1[0], s0[1], s1[1]};
      }
    }
  } else if (ROLE == 2) {
    float mean[MT], rsd[MT];
    f32x4 cg4[3], cu4[3];
#pragma unroll
    for (int i = 0; i < MT; ++i) { int r = wm * 64 + (i ^ wn) * 16 + l15; mean[i] = smean[r]; rsd[i] = srsd[r]; }
#pragma unroll
    for (int j = 0; j < 3; ++j) {
      cg4[j] = *(const f32x4*)(scs + (wn * 3 + j) * 16 + 4 * g);
      cu4[j] = *(const f32x4*)(scs + 96 + (wn * 3 + j) * 16 + 4 * g);
    }
    char* wb = (char*)outp + (size_t)((rb * 16 + wm * 4) * 54 + cgl * 3) * 1024 +
               (unsigned)(((g & 1) ^ wn) * 54 * 1024 + wn * 3 * 512 + (g >> 1) * 256 + l15 * 16);
    __builtin_amdgcn_sched_barrier(0);
#pragma unroll
    for (int ip = 0; ip < MT; ip += 2) {
#pragma unroll
      for (int j = 0; j < 3; ++j) {
        f32x2 gv[4], uv[4], sg[4];
#pragma unroll
        for (int n = 0; n < 4; ++n) {
          const int i = ip + (n >> 1), e = 2 * (n & 1);
          gv[n] = ((f32x2){acc[i][j][e], acc[i][j][e + 1]} - mean[i] * (f32x2){cg4[j][e], cg4[j][e + 1]}) * rsd[i];
        }
#pragma unroll
        for (int n = 0; n < 4; ++n) {
          const int i = ip + (n >> 1), e = 2 * (n & 1);
          uv[n] = ((f32x2){acc[i][j + 3][e], acc[i][j + 3][e + 1]} - mean[i] * (f32x2){cu4[j][e], cu4[j][e + 1]}) * rsd[i];
        }
        rbf4(gv);
#pragma unroll
        for (int n = 0; n < 4; ++n) sg[n] = gv[n] * -LOG2E;
#pragma unroll
        for (int n = 0; n < 4; ++n) sg[n] = exp2_2(sg[n]);
        rbf4(uv);
#pragma unroll
        for (int n = 0; n < 4; ++n) sg[n] = sg[n] + 1.0f;
#pragma unroll
        for (int n = 0; n < 4; ++n) sg[n] = (f32x2){__builtin_amdgcn_rcpf(sg[n][0]), __builtin_amdgcn_rcpf(sg[n][1])};
        rbf4(sg);
#pragma unroll
        for (int n = 0; n < 4; ++n) sg[n] = sg[n] * uv[n];
        const auto s0 = __builtin_amdgcn_permlane16_swap(pk2(sg[0]), pk2(sg[2]), false, false);
        const auto s1 = __builtin_amdgcn_permlane16_swap(pk2(sg[1]), pk2(sg[3]), false, false);
        *(u32x4*)(wb + ip * 54 * 1024 + j * 512) = (u32x4){s0[0], s1[0], s0[1], s1[1]};
      }
    }
  } else {
    bf16x8 fr[10];
    f32x2 mean[2][2], rsd[2][2];
    float cc[10];
#pragma unroll
    for (int i = 0; i < 2; ++i) {
      const int r = wid * 32 + i * 16 + 4 * g;
      const f32x4 m4 = *(const f32x4*)(smean + r), r4 = *(const f32x4*)(srsd + r);
      mean[i][0] = (f32x2){m4[0], m4[1]}; mean[i][1] = (f32x2){m4[2], m4[3]};
      rsd[i][0] = (f32x2){r4[0], r4[1]}; rsd[i][1] = (f32x2){r4[2], r4[3]};
    }
#pragma unroll
    for (int j = 0; j < 10; ++j) cc[j] = scs[j * 16 + l15];
    __builtin_amdgcn_sched_barrier(0);
#pragma unroll
    for (int j = 0; j < 10; ++j) {
      f32x2 t[4];
      unsigned o[4];
#pragma unroll
      for (int n = 0; n < 4; ++n) {
        const int i = n >> 1, e = n & 1;
        t[n] = ((f32x2){acc[i][j][2 * e], acc[i][j][2 * e + 1]} - mean[i][e] * cc[j]) * rsd[i][e];
      }
      if (j < 5) phi4(t, o);
      else {
#pragma unroll
        for (int n = 0; n < 4; ++n) o[n] = pk2(t[n]);
      }
      const u32x4 u = {o[0], o[1], o[2], o[3]};
      fr[j] = __builtin_bit_cast(bf16x8, u);
    }
    float* red = (float*)smem;
    float* myred = red + wid * (4 * KVSZ) + 80 * g + l15;
    const bf16x8 ones = {0x3F80, 0x3F80, 0x3F80, 0x3F80, 0x3F80, 0x3F80, 0x3F80, 0x3F80};
    int hv[5];
#pragma unroll
    for (int j = 0; j < 5; ++j) hv[j] = ((16 * j + l15) * 205) >> 12;
#pragma unroll
    for (int i = 0; i < 5; ++i) {
      const int hk = ((4 * i + g) * 52) >> 8;
#pragma unroll
      for (int j = 0; j < 5; ++j) {
        if (j < i - 1 || j > i + 1) continue;
        f32x4 z4 = {0.f, 0.f, 0.f, 0.f};
        f32x4 d = __builtin_amdgcn_mfma_f32_16x16x32_bf16(fr[i], fr[5 + j], z4, 0, 0, 0);
        if (hk == hv[j]) {
#pragma unroll
          for (int e = 0; e < 4; ++e) myred[320 * i + 20 * e + 16 * j] = d[e];
        }
      }
      f32x4 z4 = {0.f, 0.f, 0.f, 0.f};
      f32x4 d = __builtin_amdgcn_mfma_f32_16x16x32_bf16(fr[i], ones, z4, 0, 0, 0);
      if (l15 == 0) *(f32x4*)(red + wid * (4 * KVSZ) + 400 * (hk + 1) + 16 * i + 4 * g) = d;
    }
    __syncthreads();
    if (tid < KVSZ) {
      f32x4 a = *(const f32x4*)(red + 4 * tid);
#pragma unroll
      for (int w = 1; w < 8; ++w) a += *(const f32x4*)(red + w * (4 * KVSZ) + 4 * tid);
      *(f32x4*)(kvp + (size_t)(rb * NH + cgl * 4) * KVSZ + 4 * tid) = a;
    }
  }
}

__device__ __forceinline__ void k1_retile(const u16* __restrict__ wd, u16* __restrict__ wdt, int r, char* smem) {
  const int tid = threadIdx.x;
  const int wid = __builtin_amdgcn_readfirstlane(tid >> 6);
  const int lane = tid & 63;
  const int g = lane >> 4, l15 = lane & 15;
  char* area = smem + wid * 10240;
  unsigned boffs[5];
#pragma unroll
  for (int p = 0; p < 5; ++p) {
    int c = p * 64 + lane;
    int rho = c / 10, ch = c - rho * 10;
    int row = rho ^ (((rho >> 3) & 1) << 2);
    boffs[p] = (unsigned)(row * D_ + 8 * ch) * 2u;
  }
  const unsigned boffc = lds_addr(smem) + wid * 10240 + (8 * g + (l15 >> 2)) * 160 + (lane & 3) * 8;
  const unsigned boff0 = boffc + 640 * (g & 1), boff1 = boffc + 640 * ((g & 1) ^ 1);
#pragma unroll
  for (int hf = 0; hf < 2; ++hf) {
#pragma unroll
    for (int s = 0; s < 2; ++s) {
      const int idx = r * 8 + wid + 128 * (2 * hf + s);
      if (idx < 432) {
        const int cb = idx / 54, u = idx - cb * 54;
        const char* src = (const char*)(wd + (size_t)u * 32 * D_ + cb * 80);
#pragma unroll
        for (int p = 0; p < 5; ++p) glds16(src + boffs[p], area + s * 5120 + p * 1024);
      }
    }
    WAIT_V(0);
    sfor<2>([&](auto S) {
      constexpr int s = S.value;
      const int idx = r * 8 + wid + 128 * (2 * hf + s);
      if (idx < 432) {
        s16x4 wl[5], wh[5];
        sfor<5>([&](auto J) {
          wl[J.value] = tr_rd<s * 5120 + J.value * 32>(boff0);
          wh[J.value] = tr_rd<s * 5120 + J.value * 32>(boff1);
        });
        WAIT_L(0);
        char* dst = (char*)wdt + (size_t)idx * 5120 + lane * 16;
#pragma unroll
        for (int j = 0; j < 5; ++j) *(bf16x8*)(dst + j * 1024) = cat8(wl[j], wh[j]);
      }
    });
  }
}

__global__ __launch_bounds__(512, 2) void k1_kernel(const u16* __restrict__ x, const u16* __restrict__ wqkv,
                                                    const u16* __restrict__ wg, const u16* __restrict__ wu,
                                                    u16* __restrict__ pq, u16* __restrict__ h,
                                                    float* __restrict__ kvp, unsigned* __restrict__ flags,
                                                    const u16* __restrict__ wd, const u16* __restrict__ wo,
                                                    u16* __restrict__ wdt) {
  extern __shared__ __attribute__((aligned(16))) char smem[];
  const int bid = blockIdx.x;
  if (bid >= 240) { k1_retile(wd, wdt, bid - 240, smem); return; }
  if (bid == 0) flags[threadIdx.x] = 0u;
  const int lin = (bid & 7) * 30 + (bid >> 3);
  const int cg = lin >> 3, rb = lin & 7;
  if (cg < 18) k1_body<2>(x, wg, wu, h, kvp, rb, cg, (const char*)wo, smem);
  else if (cg < 26) k1_body<1>(x, wqkv, wqkv, pq, kvp, rb, cg - 18, (const char*)wo, smem);
  else k1_body<0>(x, wqkv, wqkv, pq, kvp, rb, cg - 26, (const char*)wo, smem);
}

constexpr int K2_REG = 20480;
constexpr int K2_LDS = 8 * K2_REG;
constexpr int K2_P_KV = 0;
constexpr int K2_P_WO = 13440;
constexpr int K2_P_KS = 18560;
constexpr int K2_S_ZL = 0;
constexpr int K2_S_ZS = 5120;
constexpr int K2_S_RD = 6144;

__device__ __forceinline__ unsigned gld32_sc1(const void* sbase, unsigned voff) {
  unsigned r; asm volatile("global_load_dword %0, %1, %2 sc1" : "=&v"(r) : "v"(voff), "s"(sbase) : "memory"); return r;
}
template <int OFF> __device__ __forceinline__ bf16x8 gld128_sc1(const void* sbase, unsigned voff) {
  bf16x8 r; asm volatile("global_load_dwordx4 %0, %1, %2 offset:%3 sc1" : "=&v"(r) : "v"(voff), "s"(sbase), "i"(OFF) : "memory"); return r;
}

__global__ __launch_bounds__(512, 2) void k2_kernel(const u16* __restrict__ x, const u16* __restrict__ h,
                                                    const u16* __restrict__ wdt, const u16* __restrict__ pq,
                                                    const float* __restrict__ kvp, const u16* __restrict__ wo,
                                                    u16* __restrict__ wp, unsigned* __restrict__ flags,
                                                    u16* __restrict__ out) {
  extern __shared__ __attribute__((aligned(16))) char smem[];
  const int bid = blockIdx.x;
  const int lin = (bid & 7) * 32 + (bid >> 3);
  const int rb = lin >> 3, cb = lin & 7;
  const int m0 = rb * 64, n0 = cb * 80;
  const int tid = threadIdx.x;
  const int wid = __builtin_amdgcn_readfirstlane(tid >> 6);
  const int lane = tid & 63;
  const int hi = wid >> 2;
  using I0 = std::integral_constant<int, 0>; using I1 = std::integral_constant<int, 1>; using I2 = std::integral_constant<int, 2>;
  const unsigned lane16 = (unsigned)lane * 16u;
  auto lid = [&]() { unsigned v = lane16; asm volatile("" : "+v"(v)); return (int)(v >> 4); };

  f32x4 acc[4][5];
  bf16x8 af[3][4], bf[3][5];
  const char* hbi[4];
#pragma unroll
  for (int i = 0; i < 4; ++i) hbi[i] = (const char*)h + (size_t)(rb * 4 + ((i + wid) & 3)) * 54 * 1024;
  const char* wtb = (const char*)wdt + (size_t)cb * 54 * 5120 + 2048;
  const int rot = (7 * cb + 14 * (rb & 3)) % 54;
  auto seof = [&](int q) { int s = q + rot; return s >= 54 ? s - 54 : s; };
  auto ldA = [&](int se, auto SL, auto I) { af[SL.value][I.value] = gld128a<0>(hbi[I.value] + (size_t)se * 1024, lane16); };
  auto ldB = [&](int se, auto SL, auto J, auto HI) {
    bf[SL.value][J.value] = gld128<((J.value + 3 * HI.value) % 5 - 2) * 1024>(wtb + (size_t)se * 5120, lane16);
  };
  auto ldU = [&](int se, auto SL, auto HI) {
    sfor<5>([&](auto J) { ldB(se, SL, J, HI); });
    sfor<4>([&](auto I) { ldA(se, SL, I); });
    __builtin_amdgcn_sched_barrier(0);
  };
  auto unit = [&](auto SL, auto HI, auto LD, int sen) {
    sfor<5>([&](auto J) {
      sfor<4>([&](auto I) {
        constexpr int i = I.value, j = J.value;
        acc[i][j] = __builtin_amdgcn_mfma_f32_16x16x32_bf16(bf[SL.value][j], af[SL.value][i], acc[i][j], 0, 0, 0);
        if constexpr (LD.value) {
          if constexpr (j == 4) ldA(sen, SL, I);
          if constexpr (i == 3) ldB(sen, SL, J, HI);
        }
        __builtin_amdgcn_sched_barrier(0);
      });
    });
  };
  auto zacc = [&]() {
#pragma unroll
    for (int i = 0; i < 4; ++i)
#pragma unroll
      for (int j = 0; j < 5; ++j) acc[i][j] = (f32x4){0.f, 0.f, 0.f, 0.f};
  };

  if (wid < 4) {
    const int ph = bid >> 3, pj = bid & 7;
    char* pm = smem + wid * K2_REG;
    if (wid < 2) {
      __builtin_amdgcn_s_setprio(3);
      const char* kb = (const char*)kvp + (size_t)(ph * KVSZ) * 4 + lane16;
#pragma unroll
      for (int r = 0; r < 8; ++r) {
        glds16(kb + (size_t)r * (NH * KVSZ * 4), pm + K2_P_KV + r * 1680);
        if (lane < 36) glds16(kb + (size_t)r * (NH * KVSZ * 4) + 1024, pm + K2_P_KV + r * 1680 + 1024);
      }
#pragma unroll
      for (int p = 0; p < 4; ++p) {
        const int c = lane + 64 * p, rho = c / 10, ch = c - rho * 10, e = rho ^ (((rho >> 3) & 1) << 2);
        if (c < 200) glds16(wo + (size_t)(ph * DH + e) * D_ + pj * 80 + ch * 8, pm + K2_P_WO + p * 1024);
      }
      __builtin_amdgcn_sched_barrier(0);
    } else {
      __builtin_amdgcn_s_setprio(1);
    }
    BAR();
    int q = wid < 2 ? 12 * wid : 15 * wid - 6;
    const int ng = wid < 2 ? 4 : 5;
    if (wid < 2) {
      const int sw = wid;
      const int lane = lid();
      const int g = lane >> 4, l15 = lane & 15;
      float* kvs = (float*)(pm + K2_P_KS);
      if (lane < 60) {
#pragma unroll
        for (int p = 0; p < 2; ++p) *(u32x4*)(pm + K2_P_WO + 3200 + (lane + 60 * p) * 16) = (u32x4){0u, 0u, 0u, 0u};
      }
      WAIT_VS(0);
      {
        const int l1 = min(lane, 35);
        f32x4 a0 = *(const f32x4*)(pm + K2_P_KV + lane * 16), a1 = *(const f32x4*)(pm + K2_P_KV + 1024 + l1 * 16);
#pragma unroll
        for (int r = 1; r < 8; ++r) {
          a0 += *(const f32x4*)(pm + K2_P_KV + r * 1680 + lane * 16);
          a1 += *(const f32x4*)(pm + K2_P_KV + r * 1680 + 1024 + l1 * 16);
        }
#pragma unroll
        for (int e = 0; e < 4; ++e) { a0[e] = rbf(a0[e]); a1[e] = rbf(a1[e]); }
        *(f32x4*)(kvs + 4 * lane) = a0;
        if (lane < 36) *(f32x4*)(kvs + 256 + 4 * lane) = a1;
      }
      WAIT_L(0);
      __builtin_amdgcn_wave_barrier();
      const unsigned wob = lds_addr(pm) + K2_P_WO + (8 * g + (l15 >> 2)) * 160 + (lane & 3) * 8 + (sw ? 96 : 0);
      const unsigned wo0 = wob + 640 * (g & 1), wo1 = wob + 640 * ((g & 1) ^ 1);
      s16x4 al[3], ah[3];
      sfor<3>([&](auto C) { al[C.value] = tr_rd<C.value * 32>(wo0); ah[C.value] = tr_rd<C.value * 32>(wo1); });
      bf16x8 yk[2];
#pragma unroll
      for (int t = 0; t < 2; ++t) {
        const int d = 16 * t + l15, dc = min(d, 19);
        f32x4 k0 = *(const f32x4*)(kvs + dc * DH + min(8 * g, 16)), k1 = *(const f32x4*)(kvs + dc * DH + min(8 * g + 4, 16));
        const bool v0 = d < DH && g < 3, v1 = d < DH && g < 2;
        u32x4 u = {v0 ? pack2(k0[0], k0[1]) : 0u, v0 ? pack2(k0[2], k0[3]) : 0u, v1 ? pack2(k1[0], k1[1]) : 0u, v1 ? pack2(k1[2], k1[3]) : 0u};
        yk[t] = __builtin_bit_cast(bf16x8, u);
      }
      WAIT_L(0);
      char* dst[2];
#pragma unroll
      for (int t = 0; t < 2; ++t) {
        const int kk = ph * DH + 16 * t + 4 * g;
        dst[t] = (char*)wp + (size_t)((pj * 20 + (kk >> 5)) * 5 + (sw ? 3 : 0)) * 1024 + (16 * ((kk & 31) >> 3) + l15) * 16 + ((kk & 7) >> 2) * 8;
      }
#pragma unroll
      for (int c = 0; c < 3; ++c) {
        const bf16x8 wa = cat8(al[c], ah[c]);
#pragma unroll
        for (int t = 0; t < 2; ++t) {
          const f32x4 z4 = {0.f, 0.f, 0.f, 0.f};
          f32x4 da = __builtin_amdgcn_mfma_f32_16x16x32_bf16(yk[t], wa, z4, 0, 0, 0);
          unsigned long long va = (unsigned long long)pack2(da[0], da[1]) | ((unsigned long long)pack2(da[2], da[3]) << 32);
          if (16 * t + 4 * g < DH && (c < 2 || !sw))
            __hip_atomic_store((unsigned long long*)(dst[t] + c * 1024), va, __ATOMIC_RELAXED, __HIP_MEMORY_SCOPE_AGENT);
        }
      }
      __builtin_amdgcn_sched_barrier(0);
      ldU(seof(q), I0{}, I0{});
      WAIT_VS(9);
      if (lane == 0) __hip_atomic_store(flags + (pj * 32 + ph) * 2 + sw, 1u, __ATOMIC_RELAXED, __HIP_MEMORY_SCOPE_AGENT);
      __builtin_amdgcn_s_setprio(1);
      __builtin_amdgcn_sched_barrier(0);
    } else {
      ldU(seof(q), I0{}, I0{});
    }
    ldU(seof(q + 1), I1{}, I0{});
    ldU(seof(q + 2), I2{}, I0{});
    zacc();
#pragma nounroll
    for (int gi = 1; gi < ng; ++gi) {
      WAIT_VS(18); unit(I0{}, I0{}, I1{}, seof(q + 3));
      WAIT_VS(18); unit(I1{}, I0{}, I1{}, seof(q + 4));
      WAIT_VS(18); unit(I2{}, I0{}, I1{}, seof(q + 5));
      q += 3;
    }
    WAIT_VS(18); unit(I0{}, I0{}, I0{}, 0);
    WAIT_VS(9); unit(I1{}, I0{}, I0{}, 0);
    WAIT_VS(0); unit(I2{}, I0{}, I0{}, 0);
  } else {
    __builtin_amdgcn_s_setprio(2);
    const int k = wid - 4;
    char* sm = smem + wid * K2_REG;
    {
      const char* zb = (const char*)kvp + (size_t)((8 * k + lane / 5) * KVSZ + DH * DH + 4 * (lane % 5)) * 4;
      if (lane < 40) {
#pragma unroll
        for (int r = 0; r < 8; ++r) glds16(zb + (size_t)r * (NH * KVSZ * 4), sm + K2_S_ZL + r * 640);
      }
      __builtin_amdgcn_sched_barrier(0);
    }
    const char* pqb[4];
#pragma unroll
    for (int i = 0; i < 4; ++i) pqb[i] = (const char*)pq + (size_t)((rb * 4 + ((i + wid) & 3)) * 20 + 5 * k) * 1024;
    bf16x8 pf[5][4];
#pragma unroll
    for (int i = 0; i < 4; ++i) pf[0][i] = gld128a<0>(pqb[i], lane16);
#pragma unroll
    for (int i = 0; i < 4; ++i) pf[1][i] = gld128a<1024>(pqb[i], lane16);
#pragma unroll
    for (int i = 0; i < 4; ++i) pf[2][i] = gld128a<2048>(pqb[i], lane16);
#pragma unroll
    for (int i = 0; i < 4; ++i) pf[3][i] = gld128a<3072>(pqb[i], lane16);
#pragma unroll
    for (int i = 0; i < 4; ++i) pf[4][i] = gld128a<0>(pqb[i] + 4096, lane16);
    __builtin_amdgcn_sched_barrier(0);
    BAR();
    float* zs = (float*)(sm + K2_S_ZS);
    float* rden = (float*)(sm + K2_S_RD);
    WAIT_VS(0);
#pragma unroll
    for (int u = 0; u < 5; ++u)
#pragma unroll
      for (int i = 0; i < 4; ++i) asm volatile("" : "+a"(pf[u][i]));
    __builtin_amdgcn_sched_barrier(0);
    {
      const int lane = lid();
      const int l1 = min(lane, 39);
      f32x4 z0 = *(const f32x4*)(sm + K2_S_ZL + l1 * 16);
#pragma unroll
      for (int r = 1; r < 8; ++r) z0 += *(const f32x4*)(sm + K2_S_ZL + r * 640 + l1 * 16);
#pragma unroll
      for (int e = 0; e < 4; ++e) z0[e] = rbf(z0[e]);
      *(f32x4*)(zs + 4 * lane) = z0;
    }
    WAIT_L(0);
    __builtin_amdgcn_wave_barrier();
    {
      const int lq = lid();
      const int g = lq >> 4, l15 = lq & 15;
      f32x4 dn[4];
#pragma unroll
      for (int i = 0; i < 4; ++i) dn[i] = (f32x4){0.f, 0.f, 0.f, 0.f};
#pragma unroll
      for (int u = 0; u < 5; ++u) {
        f32x4 z0 = *(const f32x4*)(zs + 32 * u + 8 * g), z1 = *(const f32x4*)(zs + 32 * u + 8 * g + 4);
        float zv[8] = {z0[0], z0[1], z0[2], z0[3], z1[0], z1[1], z1[2], z1[3]};
        const int k0 = 32 * u + 8 * g;
        const int h0 = k0 / DH, lim = (h0 + 1) * DH - k0;
        const bool m0_ = (h0 == l15), m1_ = (h0 + 1 == l15);
        u32x4 zm;
#pragma unroll
        for (int e = 0; e < 4; ++e) {
          bool m = (2 * e < lim) ? m0_ : m1_;
          zm[e] = m ? pack2(zv[2 * e], zv[2 * e + 1]) : 0u;
        }
        bf16x8 zz = __builtin_bit_cast(bf16x8, zm);
#pragma unroll
        for (int i = 0; i < 4; ++i) dn[i] = __builtin_amdgcn_mfma_f32_16x16x32_bf16(zz, pf[u][i], dn[i], 0, 0, 0);
        __builtin_amdgcn_sched_barrier(0);
      }
      if (g < 2) {
#pragma unroll
        for (int i = 0; i < 4; ++i) {
          f32x4 r;
#pragma unroll
          for (int e = 0; e < 4; ++e) r[e] = __builtin_amdgcn_rcpf(rbf(rbf(dn[i][e]) + 1e-6f));
          *(f32x4*)(rden + (i * 16 + l15) * 8 + 4 * g) = r;
        }
      }
      __builtin_amdgcn_s_waitcnt(0xc07f);
      __builtin_amdgcn_wave_barrier();
#pragma unroll
      for (int u = 0; u < 5; ++u) {
        const int k0 = 32 * u + 8 * g;
        const int h0 = k0 / DH;
        const int lim = (h0 + 1) * DH - k0;
#pragma unroll
        for (int i = 0; i < 4; ++i) {
          const float* rp = rden + (i * 16 + l15) * 8 + h0;
          float r0 = rp[0], r1 = rp[1];
          u32x4 uu = __builtin_bit_cast(u32x4, pf[u][i]);
          u32x4 o;
#pragma unroll
          for (int e = 0; e < 4; ++e) {
            float r = (2 * e < lim) ? r0 : r1;
            o[e] = pack2(bflo(uu[e]) * r, bfhi(uu[e]) * r);
          }
          pf[u][i] = __builtin_bit_cast(bf16x8, o);
        }
        __builtin_amdgcn_sched_barrier(0);
      }
      __builtin_amdgcn_s_waitcnt(0xc07f);
      __builtin_amdgcn_sched_barrier(0);
    }
    __builtin_amdgcn_s_setprio(3);
    {
      const unsigned* fp = flags + (cb * 32 + 8 * k) * 2;
      const unsigned fo = (lane16 >> 2) & 60u;
      __builtin_amdgcn_s_sleep(40);
      for (;;) {
        unsigned f = gld32_sc1(fp, fo);
        WAIT_VS(0);
        asm volatile("" : "+v"(f));
        if (__all(f == 1u)) break;
        __builtin_amdgcn_s_sleep(8);
      }
    }
    __builtin_amdgcn_sched_barrier(0);
    const char* wpb = (const char*)wp + (size_t)(cb * 20 + 5 * k) * 5120 + 2048;
    auto ldS = [&](auto U) {
      sfor<5>([&](auto J) {
        bf[U.value % 3][J.value] = gld128_sc1<((J.value + 3) % 5 - 2) * 1024>(wpb + U.value * 5120, lane16);
      });
      __builtin_amdgcn_sched_barrier(0);
    };
    ldS(I0{}); ldS(I1{}); ldS(I2{});
    zacc();
    __builtin_amdgcn_sched_barrier(0);
    sfor<5>([&](auto UU) {
      constexpr int u = UU.value;
      if constexpr (u <= 2) WAIT_VS(10); else if constexpr (u == 3) WAIT_VS(5); else WAIT_VS(0);
      sfor<5>([&](auto J) {
        sfor<4>([&](auto I) {
          constexpr int i = I.value, j = J.value;
          acc[i][j] = __builtin_amdgcn_mfma_f32_16x16x32_bf16(bf[u % 3][j], pf[u][i], acc[i][j], 0, 0, 0);
          __builtin_amdgcn_sched_barrier(0);
        });
      });
      if constexpr (u + 3 < 5) ldS(std::integral_constant<int, u + 3>{});
    });
  }

  int ln;
  asm volatile("v_mbcnt_lo_u32_b32 %0, -1, 0\n\tv_mbcnt_hi_u32_b32 %0, -1, %0" : "=v"(ln));
  const int g2 = ln >> 4;
  const unsigned orow = (unsigned)(m0 + (wid & 3) * 16 + (ln & 15));
  u32x2 xv[3];
#pragma unroll
  for (int j = 0; j < 3; ++j) xv[j] = *(const u32x2*)(x + (size_t)orow * D_ + n0 + ((j + 3 * hi) % 5) * 16 + 4 * g2);
  {
    char* myr = smem + wid * K2_REG + ln * 16;
#pragma unroll
    for (int i = 0; i < 4; ++i)
#pragma unroll
      for (int j = 0; j < 5; ++j) *(f32x4*)(myr + (((i + wid) & 3) * 5 + (j + 3 * hi) % 5) * 1024) = acc[i][j];
  }
  __syncthreads();
  f32x2 ys[6], ss[6];
#pragma unroll
  for (int n = 0; n < 6; ++n) { ys[n] = (f32x2){0.f, 0.f}; ss[n] = (f32x2){0.f, 0.f}; }
#pragma unroll
  for (int w = 0; w < 8; ++w) {
    const char* src = smem + w * K2_REG + ln * 16 + (wid & 3) * 5 * 1024;
#pragma unroll
    for (int j = 0; j < 3; ++j) {
      if (j == 2 && hi) continue;
      f32x4 a = *(const f32x4*)(src + ((j + 3 * hi) % 5) * 1024);
      const f32x2 a0 = {a[0], a[1]}, a1 = {a[2], a[3]};
      if (w == 0) { ys[2 * j] = a0; ys[2 * j + 1] = a1; }
      else if (w < 4) { ys[2 * j] += a0; ys[2 * j + 1] += a1; }
      else if (w == 4) { ss[2 * j] = a0; ss[2 * j + 1] = a1; }
      else { ss[2 * j] += a0; ss[2 * j + 1] += a1; }
    }
  }
  auto fin = [&](auto N0, auto NN) {
    constexpr int n0_ = N0.value, N = NN.value;
    f32x2 y[N], sv[N], t[N];
#pragma unroll
    for (int n = 0; n < N; ++n) { y[n] = ys[n0_ + n]; sv[n] = ss[n0_ + n]; }
    rbf4(y);
#pragma unroll
    for (int n = 0; n < N; ++n) t[n] = y[n] * -LOG2E;
#pragma unroll
    for (int n = 0; n < N; ++n) t[n] = exp2_2(t[n]);
    rbf4(sv);
#pragma unroll
    for (int n = 0; n < N; ++n) t[n] = t[n] + 1.0f;
#pragma unroll
    for (int n = 0; n < N; ++n) t[n] = (f32x2){__builtin_amdgcn_rcpf(t[n][0]), __builtin_amdgcn_rcpf(t[n][1])};
#pragma unroll
    for (int n = 0; n < N; ++n) t[n] = y[n] * t[n];
    rbf4(t);
#pragma unroll
    for (int n = 0; n < N; ++n) t[n] = sv[n] * t[n];
    rbf4(t);
#pragma unroll
    for (int jj = 0; jj < N / 2; ++jj) {
      const int j = n0_ / 2 + jj;
      const size_t o = (size_t)orow * D_ + n0 + ((j + 3 * hi) % 5) * 16 + 4 * g2;
      u32x2 ov = {pk2(up2(xv[j][0]) + t[2 * jj]), pk2(up2(xv[j][1]) + t[2 * jj + 1])};
      *(u32x2*)(out + o) = ov;
    }
  };
  fin(std::integral_constant<int, 0>{}, std::integral_constant<int, 4>{});
  if (!hi) fin(std::integral_constant<int, 4>{}, std::integral_constant<int, 2>{});
}

extern "C" void kernel_launch(void* const* d_in, const int* in_sizes, int n_in, void* d_out, int out_size,
                              void* d_ws, size_t ws_size, hipStream_t stream) {
  const u16* x = (const u16*)d_in[0];
  const u16* wqkv = (const u16*)d_in[1];
  const u16* wo = (const u16*)d_in[2];
  const u16* wg = (const u16*)d_in[4];
  const u16* wu = (const u16*)d_in[5];
  const u16* wd = (const u16*)d_in[6];
  char* ws = (char*)d_ws;
  u16* pq = (u16*)(ws);
  u16* h = (u16*)(ws + (4 << 20));
  float* kvp = (float*)(ws + (12 << 20));
  u16* wp = (u16*)(ws + (13 << 20));
  unsigned* flags = (unsigned*)(ws + (14 << 20));
  u16* wdt = (u16*)(ws + (16 << 20));
  (void)hipFuncSetAttribute((const void*)k1_kernel, hipFuncAttributeMaxDynamicSharedMemorySize, K1_LDS);
  (void)hipFuncSetAttribute((const void*)k2_kernel, hipFuncAttributeMaxDynamicSharedMemorySize, K2_LDS);
  k1_kernel<<<256, 512, K1_LDS, stream>>>(x, wqkv, wg, wu, pq, h, kvp, flags, wd, wo, wdt);
  k2_kernel<<<256, 512, K2_LDS, stream>>>(x, h, wdt, pq, kvp, wo, wp, flags, (u16*)d_out);
}
```

```cpp
#include <hip/hip_runtime.h>
#include <hip/hip_bf16.h>
#include <stdint.h>
#include <utility>

typedef unsigned short u16;
typedef __attribute__((ext_vector_type(8))) short bf16x8;
typedef __attribute__((ext_vector_type(4))) short s16x4;
typedef __attribute__((ext_vector_type(4))) float f32x4;
typedef __attribute__((ext_vector_type(2))) __bf16 bf2;
typedef __attribute__((ext_vector_type(4))) unsigned u32x4;
typedef __attribute__((ext_vector_type(2))) unsigned u32x2;
#define LAS __attribute__((address_space(3)))

constexpr int S_ = 2048, D_ = 640, F_ = 1728, NH = 32, DH = 20;
constexpr int KVSZ = DH * DH + DH;

__device__ __forceinline__ unsigned pack2(float a, float b) {
  bf2 v = {(__bf16)a, (__bf16)b};
  return __builtin_bit_cast(unsigned, v);
}
__device__ __forceinline__ float rbf(float a) { return (float)(__bf16)a; }
__device__ __forceinline__ float bflo(unsigned u) { return __uint_as_float(u << 16); }
__device__ __forceinline__ float bfhi(unsigned u) { return __uint_as_float(u & 0xffff0000u); }
__device__ __forceinline__ float dot_ones(unsigned a, float c) {
  bf2 one = {(__bf16)1.0f, (__bf16)1.0f};
  return __builtin_amdgcn_fdot2_f32_bf16(__builtin_bit_cast(bf2, a), one, c, false);
}
__device__ __forceinline__ float dot_self(unsigned a, float c) {
  bf2 v = __builtin_bit_cast(bf2, a);
  return __builtin_amdgcn_fdot2_f32_bf16(v, v, c, false);
}
typedef __attribute__((ext_vector_type(2))) float f32x2;
__device__ __forceinline__ f32x2 up2(unsigned p) { return (f32x2){bflo(p), bfhi(p)}; }
__device__ __forceinline__ unsigned pk2(f32x2 v) { return pack2(v[0], v[1]); }
__device__ __forceinline__ f32x2 rbf2(f32x2 v) {
  unsigned p = pk2(v); asm("" : "+v"(p)); return up2(p);
}
__device__ __forceinline__ f32x2 exp2_2(f32x2 a) { return (f32x2){__builtin_amdgcn_exp2f(a[0]), __builtin_amdgcn_exp2f(a[1])}; }
constexpr float LOG2E = 1.44269504088896340736f;
__device__ __forceinline__ void rbf4(f32x2 (&v)[4]) {
  unsigned p[4];
#pragma unroll
  for (int n = 0; n < 4; ++n) p[n] = pk2(v[n]);
  asm("" : "+v"(p[0]), "+v"(p[1]), "+v"(p[2]), "+v"(p[3]));
#pragma unroll
  for (int n = 0; n < 4; ++n) v[n] = up2(p[n]);
}
__device__ __forceinline__ void rbf4(f32x2 (&v)[2]) {
  unsigned p0 = pk2(v[0]), p1 = pk2(v[1]);
  asm("" : "+v"(p0), "+v"(p1));
  v[0] = up2(p0); v[1] = up2(p1);
}
__device__ __forceinline__ float max0(float x) { float r; asm("v_max_f32 %0, 0, %1" : "=v"(r) : "v"(x)); return r; }
__device__ __forceinline__ void phi4(f32x2 (&t)[4], unsigned (&o)[4]) {
  rbf4(t);
  f32x2 ex[4], p[4];
#pragma unroll
  for (int n = 0; n < 4; ++n) ex[n] = t[n] * LOG2E;
#pragma unroll
  for (int n = 0; n < 4; ++n) {
    ex[n] = exp2_2(ex[n]);
    ex[n] = (f32x2){__builtin_amdgcn_fmed3f(ex[n][0], 0.f, 1.f), __builtin_amdgcn_fmed3f(ex[n][1], 0.f, 1.f)};
  }
#pragma unroll
  for (int n = 0; n < 4; ++n) p[n] = (f32x2){max0(t[n][0]), max0(t[n][1])};
#pragma unroll
  for (int n = 0; n < 4; ++n) p[n] = p[n] + ex[n];
  rbf4(p);
#pragma unroll
  for (int n = 0; n < 4; ++n) p[n] = p[n] + 1e-6f;
#pragma unroll
  for (int n = 0; n < 4; ++n) o[n] = pk2(p[n]);
}
__device__ __forceinline__ void glds16(const void* g, void* l) {
  __builtin_amdgcn_global_load_lds((const unsigned*)g, (unsigned*)l, 16, 0, 0);
}
template <class F, int... I> __device__ __forceinline__ void sfor_impl(F&& f, std::integer_sequence<int, I...>) {
  (f(std::integral_constant<int, I>{}), ...);
}
template <int N, class F> __device__ __forceinline__ void sfor(F&& f) { sfor_impl(f, std::make_integer_sequence<int, N>{}); }
#define WAIT_V(n) asm volatile("s_waitcnt vmcnt(" #n ")" ::: "memory")
#define WAIT_VS(n) do { WAIT_V(n); __builtin_amdgcn_sched_barrier(0); } while (0)
#define BAR() __builtin_amdgcn_s_barrier()
template <int OFF> __device__ __forceinline__ s16x4 tr_rd(unsigned a) {
  s16x4 r; asm volatile("ds_read_b64_tr_b16 %0, %1 offset:%2" : "=&v"(r) : "v"(a), "i"(OFF) : "memory"); return r;
}
template <int OFF> __device__ __forceinline__ bf16x8 lds_rd128(unsigned a) {
  bf16x8 r; asm volatile("ds_read_b128 %0, %1 offset:%2" : "=&v"(r) : "v"(a), "i"(OFF) : "memory"); return r;
}
template <int OFF> __device__ __forceinline__ bf16x8 gld128(const void* sbase, unsigned voff) {
  bf16x8 r; asm volatile("global_load_dwordx4 %0, %1, %2 offset:%3" : "=&v"(r) : "v"(voff), "s"(sbase), "i"(OFF) : "memory"); return r;
}
template <int OFF> __device__ __forceinline__ bf16x8 gld128a(const void* sbase, unsigned voff) {
  bf16x8 r; asm volatile("global_load_dwordx4 %0, %1, %2 offset:%3" : "=&a"(r) : "v"(voff), "s"(sbase), "i"(OFF) : "memory"); return r;
}
__device__ __forceinline__ bf16x8 cat8(s16x4 a, s16x4 b) { return __builtin_shufflevector(a, b, 0, 1, 2, 3, 4, 5, 6, 7); }
#define WAIT_L(n) do { asm volatile("s_waitcnt lgkmcnt(" #n ")" ::: "memory"); __builtin_amdgcn_sched_barrier(0); } while (0)
__device__ __forceinline__ unsigned lds_addr(const void* p) { return (unsigned)(size_t)(LAS const char*)p; }
__device__ __forceinline__ float phi_bf(float t) {
  float p = t > 0.f ? rbf(t + 1.0f) : rbf(__expf(t));
  return rbf(p + 1e-6f);
}

constexpr int K1_ABYTES = 256 * 64 * 2;
constexpr int K1_BBYTES = 64 * 192 * 2;
constexpr int K1_OFF_B = 3 * K1_ABYTES;
constexpr int K1_OFF_MISC = K1_OFF_B + 2 * K1_BBYTES;
constexpr int K1_OFF_DUMMY = K1_OFF_MISC;
constexpr int K1_OFF_MEAN = K1_OFF_DUMMY + 1024;
constexpr int K1_OFF_CSF = K1_OFF_MEAN + 2048;
constexpr int K1_LDS = K1_OFF_CSF + 768;
constexpr int K1_OFF_RS = K1_ABYTES;
constexpr int K1_OFF_CS = 2 * K1_ABYTES;

template <int ROLE>
__device__ __forceinline__ void k1_body(const u16* __restrict__ x, const u16* __restrict__ w0,
                                        const u16* __restrict__ w1, u16* __restrict__ outp,
                                        float* __restrict__ kvp, int rb, int cgl,
                                        const char* __restrict__ wop, char* smem) {
  constexpr int BN = ROLE == 2 ? 192 : 160;
  constexpr int NB = BN / 16;
  constexpr int WM = ROLE == 1 ? 8 : 4;
  constexpr int WN = 8 / WM;
  constexpr int MT = 16 / WM;
  constexpr int NT = NB / WN;
  constexpr int BPIECES = 64 * BN * 2 / 1024;
  constexpr int LDW = ROLE == 2 ? F_ : 3 * D_;
  constexpr int RBI = ROLE == 2 ? 192 : ROLE == 0 ? 320 : 160;
  constexpr int CPR = RBI / 16;
  constexpr int PPI = 64 * RBI / 1024;
  constexpr int IMGB = 64 * RBI;

  const int tid = threadIdx.x;
  const int wid = __builtin_amdgcn_readfirstlane(tid >> 6);
  const int lane = tid & 63;
  const int g = lane >> 4, l15 = lane & 15;
  const int wm = wid % WM, wn = wid / WM;
  const int m0 = rb * 256;
  const int CSOFF = 2 * wn;

  const char* abase = (const char*)(x + (size_t)m0 * D_);
  const unsigned aoffg = (unsigned)(((wid * 8 + (lane >> 3)) * D_ + (((lane & 7) ^ (lane >> 3)) * 8)) * 2);
  const char* bptr[3];
#pragma unroll
  for (int i = 0; i < 3; ++i) {
    int p = wid + 8 * i;
    if (p >= BPIECES) p = wid;
    const int img = p / PPI, c = (p - img * PPI) * 64 + lane;
    const int rho = c / CPR, chp = c - rho * CPR;
    const int k = ROLE == 1 ? (rho ^ (((rho >> 3) & 1) << 2)) : rho;
    const int ch = ROLE == 1 ? chp : (chp ^ (2 * ((rho >> 3) & 1)));
    const u16* wp;
    if (ROLE == 0) wp = w0 + cgl * 160;
    else if (ROLE == 1) wp = w0 + (img ? 2 * D_ : D_) + cgl * 80;
    else wp = (img ? w1 : w0) + cgl * 96;
    bptr[i] = (const char*)(wp + (size_t)k * LDW + ch * 8);
  }
  auto loadA = [&](int t, int slot, int i) {
    glds16(abase + (size_t)(i * 64 * D_ * 2 + t * 128) + aoffg, smem + slot * K1_ABYTES + (wid + 8 * i) * 1024);
  };
  auto loadB = [&](int t, int slot, int i) {
    if (wid + 8 * i < BPIECES) {
      glds16(bptr[i] + (size_t)t * (64 * LDW * 2), smem + K1_OFF_B + slot * K1_BBYTES + (wid + 8 * i) * 1024);
    }
  };
  auto stageA = [&](int t, int slot) {
#pragma unroll
    for (int i = 0; i < 4; ++i) loadA(t, slot, i);
  };
  auto stageB = [&](int t, int slot) {
#pragma unroll
    for (int i = 0; i < 3; ++i) loadB(t, slot, i);
  };

  const char* pfo = wop + (size_t)((blockIdx.x * 8 + wid) % 800) * 1024 + lane * 16;
  stageA(0, 0);
  stageB(0, 0);
  stageA(1, 1);

  const int arow = wm * (MT * 16) + l15;
  const unsigned lbase = lds_addr(smem);
  const unsigned aoff0 = lbase + arow * 128 + ((g ^ (lane & 7)) * 16);
  const int bq = l15 >> 2, bp = lane & 3;
  constexpr int NBJ = ROLE == 2 ? 3 : ROLE == 0 ? 5 : 2;
  unsigned bj[NBJ];
  if (ROLE == 1) {
    bj[0] = lbase + K1_OFF_B + (8 * g + bq + 4 * (g & 1)) * RBI + bp * 8;
    bj[1] = lbase + K1_OFF_B + (8 * g + bq + 4 * ((g & 1) ^ 1)) * RBI + bp * 8;
  } else {
#pragma unroll
    for (int j = 0; j < NBJ; ++j) bj[j] = lbase + K1_OFF_B + (8 * g + bq) * RBI + (((wn * NBJ + j) ^ (g & 1)) * 32) + bp * 8;
  }

  f32x4 acc[MT][NT];
#pragma unroll
  for (int i = 0; i < MT; ++i)
#pragma unroll
    for (int j = 0; j < NT; ++j) acc[i][j] = (f32x4){0.f, 0.f, 0.f, 0.f};
  float rs[MT], rq[MT], cs[NT];
#pragma unroll
  for (int i = 0; i < MT; ++i) { rs[i] = 0.f; rq[i] = 0.f; }
#pragma unroll
  for (int j = 0; j < NT; ++j) cs[j] = 0.f;

  bf16x8 xf[2][MT];
  s16x4 wl[2][NT], wh[2][NT];
  auto rd = [&](auto S, unsigned soa, unsigned sob) {
    constexpr int s = S.value;
    const unsigned aa = (aoff0 ^ (s * 64)) + soa;
    sfor<MT>([&](auto I) { xf[s][I.value] = lds_rd128<I.value * 2048>((I.value & 1) ? aa - wn * 2048 : aa + wn * 2048); });
    unsigned bs[NBJ];
#pragma unroll
    for (int j = 0; j < NBJ; ++j) bs[j] = bj[j] + sob;
    sfor<NT>([&](auto J) {
      constexpr int j = J.value;
      if constexpr (ROLE == 1) {
        constexpr int off = (j / 5) * IMGB + s * 32 * RBI + (j % 5) * 32;
        wl[s][j] = tr_rd<off>(bs[0]);
        wh[s][j] = tr_rd<off>(bs[1]);
      } else {
        constexpr int off = (j / NBJ) * IMGB + s * 32 * RBI;
        wl[s][j] = tr_rd<off>(bs[j % NBJ]);
        wh[s][j] = tr_rd<off + 4 * RBI>(bs[j % NBJ]);
      }
    });
  };
  auto rd1 = [&](auto S, auto R, unsigned aa, const unsigned* bs) {
    constexpr int s = S.value, r = R.value;
    if constexpr (r < MT) {
      xf[s][r] = lds_rd128<r * 2048>((r & 1) ? aa - wn * 2048 : aa + wn * 2048);
    } else {
      constexpr int j = (r - MT) / 2, hi = (r - MT) & 1;
      if constexpr (ROLE == 1) {
        constexpr int off = (j / 5) * IMGB + s * 32 * RBI + (j % 5) * 32;
        if constexpr (hi) wh[s][j] = tr_rd<off>(bs[1]); else wl[s][j] = tr_rd<off>(bs[0]);
      } else {
        constexpr int off = (j / NBJ) * IMGB + s * 32 * RBI;
        if constexpr (hi) wh[s][j] = tr_rd<off + 4 * RBI>(bs[j % NBJ]); else wl[s][j] = tr_rd<off>(bs[j % NBJ]);
      }
    }
  };
  auto mm = [&](auto S, auto LDX, int t, int nslot, bool do_cs, auto RSX, unsigned soa, unsigned sob) {
    constexpr int s = S.value, LD = LDX.value, RS = RSX.value;
    constexpr int NRDX = MT + 2 * NT;
    const unsigned aa = (aoff0 ^ ((RS & 1) * 64)) + soa;
    unsigned bs[NBJ];
#pragma unroll
    for (int j = 0; j < NBJ; ++j) bs[j] = bj[j] + sob;
    bf16x8 wf[NT];
#pragma unroll
    for (int j = 0; j < NT; ++j) wf[j] = cat8(wl[s][j], wh[s][j]);
    constexpr int NM = MT * NT;
    constexpr int ND = (WN == 2) ? (MT / 2) * 8 : MT * 8;
    sfor<NM>([&](auto X) {
      constexpr int xi = X.value, i = xi / NT, j = xi % NT;
      if (ROLE == 1) acc[i][j] = __builtin_amdgcn_mfma_f32_16x16x32_bf16(xf[s][i], wf[j], acc[i][j], 0, 0, 0);
      else acc[i][j] = __builtin_amdgcn_mfma_f32_16x16x32_bf16(wf[j], xf[s][i], acc[i][j], 0, 0, 0);
      constexpr int d0 = xi * ND / NM, d1 = (xi + 1) * ND / NM;
      sfor<d1 - d0>([&](auto Y) {
        constexpr int d = d0 + Y.value;
        constexpr int NTL = ND / 8;
        constexpr int q = (d >> 1) % NTL, e = (d >> 1) / NTL;
        constexpr int ii = (WN == 2) ? 2 * q : q;
        unsigned u = __builtin_bit_cast(u32x4, xf[s][ii])[e];
        if (d & 1) rq[q] = dot_self(u, rq[q]); else rs[q] = dot_ones(u, rs[q]);
      });
      if constexpr (RS < 2) {
        constexpr int NMR = (NM * 3) / 4;
        constexpr int r0 = xi < NMR ? xi * NRDX / NMR : NRDX, r1 = xi + 1 < NMR ? (xi + 1) * NRDX / NMR : NRDX;
        sfor<r1 - r0>([&](auto Y) { rd1(std::integral_constant<int, RS & 1>{}, std::integral_constant<int, r0 + Y.value>{}, aa, bs); });
      }
      constexpr int P = NM / 4;
      if constexpr (LD == 0 && (xi == 1 || xi == 1 + P || xi == 1 + 2 * P)) { if (t + 1 < 10) loadB(t + 1, (t + 1) & 1, (xi - 1) / P); else if (xi == 1) glds16(pfo, smem + K1_OFF_DUMMY); }
      if constexpr (LD == 0 && xi == 1 + 3 * P) { if (t + 2 < 10) loadA(t + 2, nslot, 0); }
      if constexpr (LD == 1 && (xi == 1 || xi == 1 + P || xi == 1 + 2 * P)) {
        if (t + 2 < 10) loadA(t + 2, nslot, 1 + (xi - 1) / P);
      }
      __builtin_amdgcn_sched_barrier(0);
    });
    if (do_cs) {
#pragma unroll
      for (int j = 0; j < NT; ++j) {
        u32x4 u = __builtin_bit_cast(u32x4, wf[j]);
#pragma unroll
        for (int e = 0; e < 4; ++e) cs[j] = dot_ones(u[e], cs[j]);
      }
    }
  };
  using I0 = std::integral_constant<int, 0>; using I1 = std::integral_constant<int, 1>; using I2 = std::integral_constant<int, 2>;
  constexpr int NRD = MT + 2 * NT;

  int aslot = 0;
  bool last_cs;
  {
    bool prev_cs = (CSOFF % WM) == wm;
    {
      WAIT_V(4);
      BAR();
      rd(I0{}, 0u, 0u);
      stageB(1, 1);
      loadA(2, 2, 0);
      WAIT_L(0);
      mm(I0{}, I1{}, 0, 2, prev_cs, I1{}, 0u, 0u);
      WAIT_L(0);
      aslot = 1;
    }
#pragma nounroll
    for (int t = 1; t < 10; ++t) {
      if (t < 9) WAIT_V(4); else WAIT_V(0);
      BAR();
      const int nslot = aslot == 0 ? 2 : aslot - 1;
      const unsigned soa = aslot * K1_ABYTES, sob = (t & 1) * K1_BBYTES;
      aslot = aslot == 2 ? 0 : aslot + 1;
      const bool do_cs = ((t + CSOFF) % WM) == wm;
      mm(I1{}, I0{}, t, nslot, prev_cs, I0{}, soa, sob);
      WAIT_L(0);
      mm(I0{}, I1{}, t, nslot, do_cs, I1{}, soa, sob);
      WAIT_L(0);
      prev_cs = do_cs;
    }
    last_cs = prev_cs;
  }

  float* rsum = (float*)(smem + K1_OFF_RS);
  float* csum = (float*)(smem + K1_OFF_CS);
  float* smean = (float*)(smem + K1_OFF_MEAN);
  float* srsd = smean + 256;
  float* scs = (float*)(smem + K1_OFF_CSF);
  {
    bf16x8 wf[NT];
#pragma unroll
    for (int j = 0; j < NT; ++j) wf[j] = cat8(wl[1][j], wh[1][j]);
#pragma unroll
    for (int e = 0; e < 4; ++e)
#pragma unroll
      for (int q = 0; q < (WN == 2 ? MT / 2 : MT); ++q) {
        const unsigned u = __builtin_bit_cast(u32x4, xf[1][(WN == 2) ? 2 * q : q])[e];
        rs[q] = dot_ones(u, rs[q]);
        rq[q] = dot_self(u, rq[q]);
      }
    if (last_cs) {
#pragma unroll
      for (int e = 0; e < 4; ++e)
#pragma unroll
        for (int j = 0; j < NT; ++j) cs[j] = dot_ones(__builtin_bit_cast(u32x4, wf[j])[e], cs[j]);
    }
#pragma unroll
    for (int q = 0; q < (WN == 2 ? MT / 2 : MT); ++q) {
      int r = wm * (MT * 16) + ((WN == 2) ? ((2 * q) ^ wn) : q) * 16 + l15;
      float2 v = {rs[q], rq[q]};
      *(float2*)(rsum + (g * 256 + r) * 2) = v;
    }
#pragma unroll
    for (int j = 0; j < NT; ++j) {
      int nb = (ROLE == 2) ? ((j < 3) ? (wn * 3 + j) : (6 + wn * 3 + (j - 3))) : (wn * NT + j);
      csum[(wm * 4 + g) * 192 + nb * 16 + l15] = cs[j];
    }
    WAIT_L(0);
    BAR();
    constexpr int NP = WM * 4;
    float pr[NP];
    const int cn = (tid - 256 < BN) ? tid - 256 : 0;
    if (wid < 4) {
#pragma unroll
      for (int w = 0; w < 4; ++w) { float2 v = *(const float2*)(rsum + (w * 256 + tid) * 2); pr[2 * w] = v.x; pr[2 * w + 1] = v.y; }
#pragma unroll
      for (int w = 8; w < NP; ++w) pr[w] = 0.f;
    } else {
#pragma unroll
      for (int w = 0; w < NP; ++w) pr[w] = csum[w * 192 + cn];
    }
    __builtin_amdgcn_sched_barrier(0);
    constexpr int NM = MT * NT;
    auto mf = [&](auto LO, auto HI) {
      sfor<HI.value - LO.value>([&](auto X) {
        constexpr int xi = LO.value + X.value, i = xi / NT, j = xi % NT;
        if (ROLE == 1) acc[i][j] = __builtin_amdgcn_mfma_f32_16x16x32_bf16(xf[1][i], wf[j], acc[i][j], 0, 0, 0);
        else acc[i][j] = __builtin_amdgcn_mfma_f32_16x16x32_bf16(wf[j], xf[1][i], acc[i][j], 0, 0, 0);
      });
      __builtin_amdgcn_sched_barrier(0);
    };
    mf(I0{}, std::integral_constant<int, NM / 3>{});
    if (wid < 4) {
      float a = 0.f, b = 0.f;
#pragma unroll
      for (int w = 0; w < 4; ++w) { a += pr[2 * w]; b += pr[2 * w + 1]; }
      float mean = a * (1.0f / D_);
      float var = fmaxf(b * (1.0f / D_) - mean * mean, 0.f);
      float sd = rbf(sqrtf(rbf(rbf(var) + 1e-5f)));
      smean[tid] = rbf(mean);
      srsd[tid] = 1.0f / sd;
    } else {
      float a = 0.f;
#pragma unroll
      for (int w = 0; w < NP; ++w) a += pr[w];
      if (tid - 256 < BN) scs[cn] = a;
    }
    __builtin_amdgcn_sched_barrier(0);
    mf(std::integral_constant<int, NM / 3>{}, std::integral_constant<int, NM>{});
  }
  WAIT_L(0);
  BAR();

  if (ROLE == 0) {
    float mean[MT], rsd[MT];
    f32x4 c4[NT];
#pragma unroll
    for (int i = 0; i < MT; ++i) { int r = wm * 64 + (i ^ wn) * 16 + l15; mean[i] = smean[r]; rsd[i] = srsd[r]; }
#pragma unroll
    for (int j = 0; j < NT; ++j) c4[j] = *(const f32x4*)(scs + (wn * NT + j) * 16 + 4 * g);
    char* wb = (char*)outp + (size_t)((rb * 16 + wm * 4) * 20 + cgl * 5) * 1024 +
               (unsigned)(((g & 1) ^ wn) * 20 * 1024 + wn * 5 * 512 + (g >> 1) * 256 + l15 * 16);
    __builtin_amdgcn_sched_barrier(0);
#pragma unroll
    for (int ip = 0; ip < MT; ip += 2) {
#pragma unroll
      for (int j = 0; j < NT; ++j) {
        f32x2 t[4];
        unsigned o[4];
#pragma unroll
        for (int n = 0; n < 4; ++n) {
          const int i = ip + (n >> 1), e = 2 * (n & 1);
          t[n] = ((f32x2){acc[i][j][e], acc[i][j][e + 1]} - mean[i] * (f32x2){c4[j][e], c4[j][e + 1]}) * rsd[i];
        }
        phi4(t, o);
        const auto s0 = __builtin_amdgcn_permlane16_swap(o[0], o[2], false, false);
        const auto s1 = __builtin_amdgcn_permlane16_swap(o[1], o[3], false, false);
        *(u32x4*)(wb + ip * 20 * 1024 + j * 512) = (u32x4){s0[0], s1[0], s0[1], s1[1]};
      }
    }
  } else if (ROLE == 2) {
    float mean[MT], rsd[MT];
    f32x4 cg4[3], cu4[3];
#pragma unroll
    for (int i = 0; i < MT; ++i) { int r = wm * 64 + (i ^ wn) * 16 + l15; mean[i] = smean[r]; rsd[i] = srsd[r]; }
#pragma unroll
    for (int j = 0; j < 3; ++j) {
      cg4[j] = *(const f32x4*)(scs + (wn * 3 + j) * 16 + 4 * g);
      cu4[j] = *(const f32x4*)(scs + 96 + (wn * 3 + j) * 16 + 4 * g);
    }
    char* wb = (char*)outp + (size_t)((rb * 16 + wm * 4) * 54 + cgl * 3) * 1024 +
               (unsigned)(((g & 1) ^ wn) * 54 * 1024 + wn * 3 * 512 + (g >> 1) * 256 + l15 * 16);
    __builtin_amdgcn_sched_barrier(0);
#pragma unroll
    for (int ip = 0; ip < MT; ip += 2) {
#pragma unroll
      for (int j = 0; j < 3; ++j) {
        f32x2 gv[4], uv[4], sg[4];
#pragma unroll
        for (int n = 0; n < 4; ++n) {
          const int i = ip + (n >> 1), e = 2 * (n & 1);
          gv[n] = ((f32x2){acc[i][j][e], acc[i][j][e + 1]} - mean[i] * (f32x2){cg4[j][e], cg4[j][e + 1]}) * rsd[i];
        }
#pragma unroll
        for (int n = 0; n < 4; ++n) {
          const int i = ip + (n >> 1), e = 2 * (n & 1);
          uv[n] = ((f32x2){acc[i][j + 3][e], acc[i][j + 3][e + 1]} - mean[i] * (f32x2){cu4[j][e], cu4[j][e + 1]}) * rsd[i];
        }
        rbf4(gv);
#pragma unroll
        for (int n = 0; n < 4; ++n) sg[n] = gv[n] * -LOG2E;
#pragma unroll
        for (int n = 0; n < 4; ++n) sg[n] = exp2_2(sg[n]);
        rbf4(uv);
#pragma unroll
        for (int n = 0; n < 4; ++n) sg[n] = sg[n] + 1.0f;
#pragma unroll
        for (int n = 0; n < 4; ++n) sg[n] = (f32x2){__builtin_amdgcn_rcpf(sg[n][0]), __builtin_amdgcn_rcpf(sg[n][1])};
        rbf4(sg);
#pragma unroll
        for (int n = 0; n < 4; ++n) sg[n] = sg[n] * uv[n];
        const auto s0 = __builtin_amdgcn_permlane16_swap(pk2(sg[0]), pk2(sg[2]), false, false);
        const auto s1 = __builtin_amdgcn_permlane16_swap(pk2(sg[1]), pk2(sg[3]), false, false);
        *(u32x4*)(wb + ip * 54 * 1024 + j * 512) = (u32x4){s0[0], s1[0], s0[1], s1[1]};
      }
    }
  } else {
    bf16x8 fr[10];
    f32x2 mean[2][2], rsd[2][2];
    float cc[10];
#pragma unroll
    for (int i = 0; i < 2; ++i) {
      const int r = wid * 32 + i * 16 + 4 * g;
      const f32x4 m4 = *(const f32x4*)(smean + r), r4 = *(const f32x4*)(srsd + r);
      mean[i][0] = (f32x2){m4[0], m4[1]}; mean[i][1] = (f32x2){m4[2], m4[3]};
      rsd[i][0] = (f32x2){r4[0], r4[1]}; rsd[i][1] = (f32x2){r4[2], r4[3]};
    }
#pragma unroll
    for (int j = 0; j < 10; ++j) cc[j] = scs[j * 16 + l15];
    __builtin_amdgcn_sched_barrier(0);
#pragma unroll
    for (int j = 0; j < 10; ++j) {
      f32x2 t[4];
      unsigned o[4];
#pragma unroll
      for (int n = 0; n < 4; ++n) {
        const int i = n >> 1, e = n & 1;
        t[n] = ((f32x2){acc[i][j][2 * e], acc[i][j][2 * e + 1]} - mean[i][e] * cc[j]) * rsd[i][e];
      }
      if (j < 5) phi4(t, o);
      else {
#pragma unroll
        for (int n = 0; n < 4; ++n) o[n] = pk2(t[n]);
      }
      const u32x4 u = {o[0], o[1], o[2], o[3]};
      fr[j] = __builtin_bit_cast(bf16x8, u);
    }
    float* red = (float*)smem;
    float* myred = red + wid * (4 * KVSZ) + 80 * g + l15;
    const bf16x8 ones = {0x3F80, 0x3F80, 0x3F80, 0x3F80, 0x3F80, 0x3F80, 0x3F80, 0x3F80};
    int hv[5];
#pragma unroll
    for (int j = 0; j < 5; ++j) hv[j] = ((16 * j + l15) * 205) >> 12;
#pragma unroll
    for (int i = 0; i < 5; ++i) {
      const int hk = ((4 * i + g) * 52) >> 8;
#pragma unroll
      for (int j = 0; j < 5; ++j) {
        if (j < i - 1 || j > i + 1) continue;
        f32x4 z4 = {0.f, 0.f, 0.f, 0.f};
        f32x4 d = __builtin_amdgcn_mfma_f32_16x16x32_bf16(fr[i], fr[5 + j], z4, 0, 0, 0);
        if (hk == hv[j]) {
#pragma unroll
          for (int e = 0; e < 4; ++e) myred[320 * i + 20 * e + 16 * j] = d[e];
        }
      }
      f32x4 z4 = {0.f, 0.f, 0.f, 0.f};
      f32x4 d = __builtin_amdgcn_mfma_f32_16x16x32_bf16(fr[i], ones, z4, 0, 0, 0);
      if (l15 == 0) *(f32x4*)(red + wid * (4 * KVSZ) + 400 * (hk + 1) + 16 * i + 4 * g) = d;
    }
    __syncthreads();
    if (tid < KVSZ) {
      f32x4 a = *(const f32x4*)(red + 4 * tid);
#pragma unroll
      for (int w = 1; w < 8; ++w) a += *(const f32x4*)(red + w * (4 * KVSZ) + 4 * tid);
      *(f32x4*)(kvp + (size_t)(rb * NH + cgl * 4) * KVSZ + 4 * tid) = a;
    }
  }
}

__device__ __forceinline__ void k1_retile(const u16* __restrict__ wd, u16* __restrict__ wdt, int r, char* smem) {
  const int tid = threadIdx.x;
  const int wid = __builtin_amdgcn_readfirstlane(tid >> 6);
  const int lane = tid & 63;
  const int g = lane >> 4, l15 = lane & 15;
  char* area = smem + wid * 10240;
  unsigned boffs[5];
#pragma unroll
  for (int p = 0; p < 5; ++p) {
    int c = p * 64 + lane;
    int rho = c / 10, ch = c - rho * 10;
    int row = rho ^ (((rho >> 3) & 1) << 2);
    boffs[p] = (unsigned)(row * D_ + 8 * ch) * 2u;
  }
  const unsigned boffc = lds_addr(smem) + wid * 10240 + (8 * g + (l15 >> 2)) * 160 + (lane & 3) * 8;
  const unsigned boff0 = boffc + 640 * (g & 1), boff1 = boffc + 640 * ((g & 1) ^ 1);
#pragma unroll
  for (int hf = 0; hf < 2; ++hf) {
#pragma unroll
    for (int s = 0; s < 2; ++s) {
      const int idx = r * 8 + wid + 128 * (2 * hf + s);
      if (idx < 432) {
        const int cb = idx / 54, u = idx - cb * 54;
        const char* src = (const char*)(wd + (size_t)u * 32 * D_ + cb * 80);
#pragma unroll
        for (int p = 0; p < 5; ++p) glds16(src + boffs[p], area + s * 5120 + p * 1024);
      }
    }
    WAIT_V(0);
    sfor<2>([&](auto S) {
      constexpr int s = S.value;
      const int idx = r * 8 + wid + 128 * (2 * hf + s);
      if (idx < 432) {
        s16x4 wl[5], wh[5];
        sfor<5>([&](auto J) {
          wl[J.value] = tr_rd<s * 5120 + J.value * 32>(boff0);
          wh[J.value] = tr_rd<s * 5120 + J.value * 32>(boff1);
        });
        WAIT_L(0);
        char* dst = (char*)wdt + (size_t)idx * 5120 + lane * 16;
#pragma unroll
        for (int j = 0; j < 5; ++j) *(bf16x8*)(dst + j * 1024) = cat8(wl[j], wh[j]);
      }
    });
  }
}

__global__ __launch_bounds__(512, 2) void k1_kernel(const u16* __restrict__ x, const u16* __restrict__ wqkv,
                                                    const u16* __restrict__ wg, const u16* __restrict__ wu,
                                                    u16* __restrict__ pq, u16* __restrict__ h,
                                                    float* __restrict__ kvp, unsigned* __restrict__ flags,
                                                    const u16* __restrict__ wd, const u16* __restrict__ wo,
                                                    u16* __restrict__ wdt) {
  extern __shared__ __attribute__((aligned(16))) char smem[];
  const int bid = blockIdx.x;
  if (bid >= 240) { k1_retile(wd, wdt, bid - 240, smem); return; }
  if (bid == 0) flags[threadIdx.x] = 0u;
  const int lin = (bid & 7) * 30 + (bid >> 3);
  const int cg = lin >> 3, rb = lin & 7;
  if (cg < 18) k1_body<2>(x, wg, wu, h, kvp, rb, cg, (const char*)wo, smem);
  else if (cg < 26) k1_body<1>(x, wqkv, wqkv, pq, kvp, rb, cg - 18, (const char*)wo, smem);
  else k1_body<0>(x, wqkv, wqkv, pq, kvp, rb, cg - 26, (const char*)wo, smem);
}

constexpr int K2_REG = 20480;
constexpr int K2_LDS = 8 * K2_REG;
constexpr int K2_P_KV = 0;
constexpr int K2_P_WO = 13440;
constexpr int K2_P_KS = 18560;
constexpr int K2_S_ZL = 0;
constexpr int K2_S_ZS = 5120;
constexpr int K2_S_RD = 6144;

__device__ __forceinline__ unsigned gld32_sc1(const void* sbase, unsigned voff) {
  unsigned r; asm volatile("global_load_dword %0, %1, %2 sc1" : "=&v"(r) : "v"(voff), "s"(sbase) : "memory"); return r;
}
template <int OFF> __device__ __forceinline__ bf16x8 gld128_sc1(const void* sbase, unsigned voff) {
  bf16x8 r; asm volatile("global_load_dwordx4 %0, %1, %2 offset:%3 sc1" : "=&v"(r) : "v"(voff), "s"(sbase), "i"(OFF) : "memory"); return r;
}

__global__ __launch_bounds__(512, 2) void k2_kernel(const u16* __restrict__ x, const u16* __restrict__ h,
                                                    const u16* __restrict__ wdt, const u16* __restrict__ pq,
                                                    const float* __restrict__ kvp, const u16* __restrict__ wo,
                                                    u16* __restrict__ wp, unsigned* __restrict__ flags,
                                                    u16* __restrict__ out) {
  extern __shared__ __attribute__((aligned(16))) char smem[];
  const int bid = blockIdx.x;
  const int lin = (bid & 7) * 32 + (bid >> 3);
  const int rb = lin >> 3, cb = lin & 7;
  const int m0 = rb * 64, n0 = cb * 80;
  const int tid = threadIdx.x;
  const int wid = __builtin_amdgcn_readfirstlane(tid >> 6);
  const int lane = tid & 63;
  const int hi = wid >> 2;
  using I0 = std::integral_constant<int, 0>; using I1 = std::integral_constant<int, 1>; using I2 = std::integral_constant<int, 2>;
  const unsigned lane16 = (unsigned)lane * 16u;
  auto lid = [&]() { unsigned v = lane16; asm volatile("" : "+v"(v)); return (int)(v >> 4); };

  f32x4 acc[4][5];
  bf16x8 af[3][4], bf[3][5];
  const char* hbi[4];
#pragma unroll
  for (int i = 0; i < 4; ++i) hbi[i] = (const char*)h + (size_t)(rb * 4 + ((i + wid) & 3)) * 54 * 1024;
  const char* wtb = (const char*)wdt + (size_t)cb * 54 * 5120 + 2048;
  const int rot = (7 * cb + 14 * (rb & 3)) % 54;
  auto seof = [&](int q) { int s = q + rot; return s >= 54 ? s - 54 : s; };
  auto ldA = [&](int se, auto SL, auto I) { af[SL.value][I.value] = gld128a<0>(hbi[I.value] + (size_t)se * 1024, lane16); };
  auto ldB = [&](int se, auto SL, auto J, auto HI) {
    bf[SL.value][J.value] = gld128<((J.value + 3 * HI.value) % 5 - 2) * 1024>(wtb + (size_t)se * 5120, lane16);
  };
  auto ldU = [&](int se, auto SL, auto HI) {
    sfor<5>([&](auto J) { ldB(se, SL, J, HI); });
    sfor<4>([&](auto I) { ldA(se, SL, I); });
    __builtin_amdgcn_sched_barrier(0);
  };
  auto unit = [&](auto SL, auto HI, auto LD, int sen) {
    sfor<5>([&](auto J) {
      sfor<4>([&](auto I) {
        constexpr int i = I.value, j = J.value;
        acc[i][j] = __builtin_amdgcn_mfma_f32_16x16x32_bf16(bf[SL.value][j], af[SL.value][i], acc[i][j], 0, 0, 0);
        if constexpr (LD.value) {
          if constexpr (j == 4) ldA(sen, SL, I);
          if constexpr (i == 3) ldB(sen, SL, J, HI);
        }
        __builtin_amdgcn_sched_barrier(0);
      });
    });
  };
  auto zacc = [&]() {
#pragma unroll
    for (int i = 0; i < 4; ++i)
#pragma unroll
      for (int j = 0; j < 5; ++j) acc[i][j] = (f32x4){0.f, 0.f, 0.f, 0.f};
  };

  if (wid < 4) {
    const int ph = bid >> 3, pj = bid & 7;
    char* pm = smem + wid * K2_REG;
    if (wid < 2) {
      __builtin_amdgcn_s_setprio(3);
      const char* kb = (const char*)kvp + (size_t)(ph * KVSZ) * 4 + lane16;
#pragma unroll
      for (int r = 0; r < 8; ++r) {
        glds16(kb + (size_t)r * (NH * KVSZ * 4), pm + K2_P_KV + r * 1680);
        if (lane < 36) glds16(kb + (size_t)r * (NH * KVSZ * 4) + 1024, pm + K2_P_KV + r * 1680 + 1024);
      }
#pragma unroll
      for (int p = 0; p < 4; ++p) {
        const int c = lane + 64 * p, rho = c / 10, ch = c - rho * 10, e = rho ^ (((rho >> 3) & 1) << 2);
        if (c < 200) glds16(wo + (size_t)(ph * DH + e) * D_ + pj * 80 + ch * 8, pm + K2_P_WO + p * 1024);
      }
      __builtin_amdgcn_sched_barrier(0);
    } else {
      __builtin_amdgcn_s_setprio(1);
    }
    BAR();
    int q = wid < 2 ? 12 * wid : 15 * wid - 6;
    const int ng = wid < 2 ? 4 : 5;
    if (wid < 2) {
      const int sw = wid;
      const int lane = lid();
      const int g = lane >> 4, l15 = lane & 15;
      float* kvs = (float*)(pm + K2_P_KS);
      if (lane < 60) {
#pragma unroll
        for (int p = 0; p < 2; ++p) *(u32x4*)(pm + K2_P_WO + 3200 + (lane + 60 * p) * 16) = (u32x4){0u, 0u, 0u, 0u};
      }
      WAIT_VS(0);
      {
        const int l1 = min(lane, 35);
        f32x4 a0 = *(const f32x4*)(pm + K2_P_KV + lane * 16), a1 = *(const f32x4*)(pm + K2_P_KV + 1024 + l1 * 16);
#pragma unroll
        for (int r = 1; r < 8; ++r) {
          a0 += *(const f32x4*)(pm + K2_P_KV + r * 1680 + lane * 16);
          a1 += *(const f32x4*)(pm + K2_P_KV + r * 1680 + 1024 + l1 * 16);
        }
#pragma unroll
        for (int e = 0; e < 4; ++e) { a0[e] = rbf(a0[e]); a1[e] = rbf(a1[e]); }
        *(f32x4*)(kvs + 4 * lane) = a0;
        if (lane < 36) *(f32x4*)(kvs + 256 + 4 * lane) = a1;
      }
      WAIT_L(0);
      __builtin_amdgcn_wave_barrier();
      const unsigned wob = lds_addr(pm) + K2_P_WO + (8 * g + (l15 >> 2)) * 160 + (lane & 3) * 8 + (sw ? 96 : 0);
      const unsigned wo0 = wob + 640 * (g & 1), wo1 = wob + 640 * ((g & 1) ^ 1);
      s16x4 al[3], ah[3];
      sfor<3>([&](auto C) { al[C.value] = tr_rd<C.value * 32>(wo0); ah[C.value] = tr_rd<C.value * 32>(wo1); });
      bf16x8 yk[2];
#pragma unroll
      for (int t = 0; t < 2; ++t) {
        const int d = 16 * t + l15, dc = min(d, 19);
        f32x4 k0 = *(const f32x4*)(kvs + dc * DH + min(8 * g, 16)), k1 = *(const f32x4*)(kvs + dc * DH + min(8 * g + 4, 16));
        const bool v0 = d < DH && g < 3, v1 = d < DH && g < 2;
        u32x4 u = {v0 ? pack2(k0[0], k0[1]) : 0u, v0 ? pack2(k0[2], k0[3]) : 0u, v1 ? pack2(k1[0], k1[1]) : 0u, v1 ? pack2(k1[2], k1[3]) : 0u};
        yk[t] = __builtin_bit_cast(bf16x8, u);
      }
      WAIT_L(0);
      char* dst[2];
#pragma unroll
      for (int t = 0; t < 2; ++t) {
        const int kk = ph * DH + 16 * t + 4 * g;
        dst[t] = (char*)wp + (size_t)((pj * 20 + (kk >> 5)) * 5 + (sw ? 3 : 0)) * 1024 + (16 * ((kk & 31) >> 3) + l15) * 16 + ((kk & 7) >> 2) * 8;
      }
#pragma unroll
      for (int c = 0; c < 3; ++c) {
        const bf16x8 wa = cat8(al[c], ah[c]);
#pragma unroll
        for (int t = 0; t < 2; ++t) {
          const f32x4 z4 = {0.f, 0.f, 0.f, 0.f};
          f32x4 da = __builtin_amdgcn_mfma_f32_16x16x32_bf16(yk[t], wa, z4, 0, 0, 0);
          unsigned long long va = (unsigned long long)pack2(da[0], da[1]) | ((unsigned long long)pack2(da[2], da[3]) << 32);
          if (16 * t + 4 * g < DH && (c < 2 || !sw))
            __hip_atomic_store((unsigned long long*)(dst[t] + c * 1024), va, __ATOMIC_RELAXED, __HIP_MEMORY_SCOPE_AGENT);
        }
      }
      __builtin_amdgcn_sched_barrier(0);
      ldU(seof(q), I0{}, I0{});
      WAIT_VS(9);
      if (lane == 0) __hip_atomic_store(flags + (pj * 32 + ph) * 2 + sw, 1u, __ATOMIC_RELAXED, __HIP_MEMORY_SCOPE_AGENT);
      __builtin_amdgcn_s_setprio(1);
      __builtin_amdgcn_sched_barrier(0);
    } else {
      ldU(seof(q), I0{}, I0{});
    }
    ldU(seof(q + 1), I1{}, I0{});
    ldU(seof(q + 2), I2{}, I0{});
    zacc();
#pragma nounroll
    for (int gi = 1; gi < ng; ++gi) {
      WAIT_VS(18); unit(I0{}, I0{}, I1{}, seof(q + 3));
      WAIT_VS(18); unit(I1{}, I0{}, I1{}, seof(q + 4));
      WAIT_VS(18); unit(I2{}, I0{}, I1{}, seof(q + 5));
      q += 3;
    }
    WAIT_VS(18); unit(I0{}, I0{}, I0{}, 0);
    WAIT_VS(9); unit(I1{}, I0{}, I0{}, 0);
    WAIT_VS(0); unit(I2{}, I0{}, I0{}, 0);
  } else {
    __builtin_amdgcn_s_setprio(2);
    const int k = wid - 4;
    char* sm = smem + wid * K2_REG;
    {
      const char* zb = (const char*)kvp + (size_t)((8 * k + lane / 5) * KVSZ + DH * DH + 4 * (lane % 5)) * 4;
      if (lane < 40) {
#pragma unroll
        for (int r = 0; r < 8; ++r) glds16(zb + (size_t)r * (NH * KVSZ * 4), sm + K2_S_ZL + r * 640);
      }
      __builtin_amdgcn_sched_barrier(0);
    }
    const char* pqb[4];
#pragma unroll
    for (int i = 0; i < 4; ++i) pqb[i] = (const char*)pq + (size_t)((rb * 4 + ((i + wid) & 3)) * 20 + 5 * k) * 1024;
    bf16x8 pf[5][4];
#pragma unroll
    for (int i = 0; i < 4; ++i) pf[0][i] = gld128a<0>(pqb[i], lane16);
#pragma unroll
    for (int i = 0; i < 4; ++i) pf[1][i] = gld128a<1024>(pqb[i], lane16);
#pragma unroll
    for (int i = 0; i < 4; ++i) pf[2][i] = gld128a<2048>(pqb[i], lane16);
#pragma unroll
    for (int i = 0; i < 4; ++i) pf[3][i] = gld128a<3072>(pqb[i], lane16);
#pragma unroll
    for (int i = 0; i < 4; ++i) pf[4][i] = gld128a<0>(pqb[i] + 4096, lane16);
    __builtin_amdgcn_sched_barrier(0);
    BAR();
    float* zs = (float*)(sm + K2_S_ZS);
    float* rden = (float*)(sm + K2_S_RD);
    WAIT_VS(0);
#pragma unroll
    for (int u = 0; u < 5; ++u)
#pragma unroll
      for (int i = 0; i < 4; ++i) asm volatile("" : "+a"(pf[u][i]));
    __builtin_amdgcn_sched_barrier(0);
    {
      const int lane = lid();
      const int l1 = min(lane, 39);
      f32x4 z0 = *(const f32x4*)(sm + K2_S_ZL + l1 * 16);
#pragma unroll
      for (int r = 1; r < 8; ++r) z0 += *(const f32x4*)(sm + K2_S_ZL + r * 640 + l1 * 16);
#pragma unroll
      for (int e = 0; e < 4; ++e) z0[e] = rbf(z0[e]);
      *(f32x4*)(zs + 4 * lane) = z0;
    }
    WAIT_L(0);
    __builtin_amdgcn_wave_barrier();
    {
      const int lq = lid();
      const int g = lq >> 4, l15 = lq & 15;
      f32x4 dn[4];
#pragma unroll
      for (int i = 0; i < 4; ++i) dn[i] = (f32x4){0.f, 0.f, 0.f, 0.f};
#pragma unroll
      for (int u = 0; u < 5; ++u) {
        f32x4 z0 = *(const f32x4*)(zs + 32 * u + 8 * g), z1 = *(const f32x4*)(zs + 32 * u + 8 * g + 4);
        float zv[8] = {z0[0], z0[1], z0[2], z0[3], z1[0], z1[1], z1[2], z1[3]};
        const int k0 = 32 * u + 8 * g;
        const int h0 = k0 / DH, lim = (h0 + 1) * DH - k0;
        const bool m0_ = (h0 == l15), m1_ = (h0 + 1 == l15);
        u32x4 zm;
#pragma unroll
        for (int e = 0; e < 4; ++e) {
          bool m = (2 * e < lim) ? m0_ : m1_;
          zm[e] = m ? pack2(zv[2 * e], zv[2 * e + 1]) : 0u;
        }
        bf16x8 zz = __builtin_bit_cast(bf16x8, zm);
#pragma unroll
        for (int i = 0; i < 4; ++i) dn[i] = __builtin_amdgcn_mfma_f32_16x16x32_bf16(zz, pf[u][i], dn[i], 0, 0, 0);
        __builtin_amdgcn_sched_barrier(0);
      }
      if (g < 2) {
#pragma unroll
        for (int i = 0; i < 4; ++i) {
          f32x4 r;
#pragma unroll
          for (int e = 0; e < 4; ++e) r[e] = __builtin_amdgcn_rcpf(rbf(rbf(dn[i][e]) + 1e-6f));
          *(f32x4*)(rden + (i * 16 + l15) * 8 + 4 * g) = r;
        }
      }
      __builtin_amdgcn_s_waitcnt(0xc07f);
      __builtin_amdgcn_wave_barrier();
#pragma unroll
      for (int u = 0; u < 5; ++u) {
        const int k0 = 32 * u + 8 * g;
        const int h0 = k0 / DH;
        const int lim = (h0 + 1) * DH - k0;
#pragma unroll
        for (int i = 0; i < 4; ++i) {
          const float* rp = rden + (i * 16 + l15) * 8 + h0;
          float r0 = rp[0], r1 = rp[1];
          u32x4 uu = __builtin_bit_cast(u32x4, pf[u][i]);
          u32x4 o;
#pragma unroll
          for (int e = 0; e < 4; ++e) {
            float r = (2 * e < lim) ? r0 : r1;
            o[e] = pack2(bflo(uu[e]) * r, bfhi(uu[e]) * r);
          }
          pf[u][i] = __builtin_bit_cast(bf16x8, o);
        }
        __builtin_amdgcn_sched_barrier(0);
      }
      __builtin_amdgcn_s_waitcnt(0xc07f);
      __builtin_amdgcn_sched_barrier(0);
    }
    __builtin_amdgcn_s_setprio(3);
    {
      const unsigned* fp = flags + (cb * 32 + 8 * k) * 2;
      const unsigned fo = (lane16 >> 2) & 60u;
      __builtin_amdgcn_s_sleep(40);
      for (;;) {
        unsigned f = gld32_sc1(fp, fo);
        WAIT_VS(0);
        asm volatile("" : "+v"(f));
        if (__all(f == 1u)) break;
        __builtin_amdgcn_s_sleep(8);
      }
    }
    __builtin_amdgcn_sched_barrier(0);
    const char* wpb = (const char*)wp + (size_t)(cb * 20 + 5 * k) * 5120 + 2048;
    auto ldS = [&](auto U) {
      sfor<5>([&](auto J) {
        bf[U.value % 3][J.value] = gld128_sc1<((J.value + 3) % 5 - 2) * 1024>(wpb + U.value * 5120, lane16);
      });
      __builtin_amdgcn_sched_barrier(0);
    };
    ldS(I0{}); ldS(I1{}); ldS(I2{});
    zacc();
    __builtin_amdgcn_sched_barrier(0);
    sfor<5>([&](auto UU) {
      constexpr int u = UU.value;
      if constexpr (u <= 2) WAIT_VS(10); else if constexpr (u == 3) WAIT_VS(5); else WAIT_VS(0);
      sfor<5>([&](auto J) {
        sfor<4>([&](auto I) {
          constexpr int i = I.value, j = J.value;
          acc[i][j] = __builtin_amdgcn_mfma_f32_16x16x32_bf16(bf[u % 3][j], pf[u][i], acc[i][j], 0, 0, 0);
          __builtin_amdgcn_sched_barrier(0);
        });
      });
      if constexpr (u + 3 < 5) ldS(std::integral_constant<int, u + 3>{});
    });
  }

  int ln;
  asm volatile("v_mbcnt_lo_u32_b32 %0, -1, 0\n\tv_mbcnt_hi_u32_b32 %0, -1, %0" : "=v"(ln));
  const int g2 = ln >> 4;
  const unsigned orow = (unsigned)(m0 + (wid & 3) * 16 + (ln & 15));
  u32x2 xv[3];
#pragma unroll
  for (int j = 0; j < 3; ++j) xv[j] = *(const u32x2*)(x + (size_t)orow * D_ + n0 + ((j + 3 * hi) % 5) * 16 + 4 * g2);
  {
    char* myr = smem + wid * K2_REG + ln * 16;
#pragma unroll
    for (int i = 0; i < 4; ++i)
#pragma unroll
      for (int j = 0; j < 5; ++j) *(f32x4*)(myr + (((i + wid) & 3) * 5 + (j + 3 * hi) % 5) * 1024) = acc[i][j];
  }
  __syncthreads();
  f32x2 ys[6], ss[6];
#pragma unroll
  for (int n = 0; n < 6; ++n) { ys[n] = (f32x2){0.f, 0.f}; ss[n] = (f32x2){0.f, 0.f}; }
#pragma unroll
  for (int w = 0; w < 8; ++w) {
    const char* src = smem + w * K2_REG + ln * 16 + (wid & 3) * 5 * 1024;
#pragma unroll
    for (int j = 0; j < 3; ++j) {
      if (j == 2 && hi) continue;
      f32x4 a = *(const f32x4*)(src + ((j + 3 * hi) % 5) * 1024);
      const f32x2 a0 = {a[0], a[1]}, a1 = {a[2], a[3]};
      if (w == 0) { ys[2 * j] = a0; ys[2 * j + 1] = a1; }
      else if (w < 4) { ys[2 * j] += a0; ys[2 * j + 1] += a1; }
      else if (w == 4) { ss[2 * j] = a0; ss[2 * j + 1] = a1; }
      else { ss[2 * j] += a0; ss[2 * j + 1] += a1; }
    }
  }
  auto fin = [&](auto N0, auto NN) {
    constexpr int n0_ = N0.value, N = NN.value;
    f32x2 y[N], sv[N], t[N];
#pragma unroll
    for (int n = 0; n < N; ++n) { y[n] = ys[n0_ + n]; sv[n] = ss[n0_ + n]; }
    rbf4(y);
#pragma unroll
    for (int n = 0; n < N; ++n) t[n] = y[n] * -LOG2E;
#pragma unroll
    for (int n = 0; n < N; ++n) t[n] = exp2_2(t[n]);
    rbf4(sv);
#pragma unroll
    for (int n = 0; n < N; ++n) t[n] = t[n] + 1.0f;
#pragma unroll
    for (int n = 0; n < N; ++n) t[n] = (f32x2){__builtin_amdgcn_rcpf(t[n][0]), __builtin_amdgcn_rcpf(t[n][1])};
#pragma unroll
    for (int n = 0; n < N; ++n) t[n] = y[n] * t[n];
    rbf4(t);
#pragma unroll
    for (int n = 0; n < N; ++n) t[n] = sv[n] * t[n];
    rbf4(t);
#pragma unroll
    for (int jj = 0; jj < N / 2; ++jj) {
      const int j = n0_ / 2 + jj;
      const size_t o = (size_t)orow * D_ + n0 + ((j + 3 * hi) % 5) * 16 + 4 * g2;
      u32x2 ov = {pk2(up2(xv[j][0]) + t[2 * jj]), pk2(up2(xv[j][1]) + t[2 * jj + 1])};
      *(u32x2*)(out + o) = ov;
    }
  };
  fin(std::integral_constant<int, 0>{}, std::integral_constant<int, 4>{});
  if (!hi) fin(std::integral_constant<int, 4>{}, std::integral_constant<int, 2>{});
}

extern "C" void kernel_launch(void* const* d_in, const int* in_sizes, int n_in, void* d_out, int out_size,
                              void* d_ws, size_t ws_size, hipStream_t stream) {
  const u16* x = (const u16*)d_in[0];
  const u16* wqkv = (const u16*)d_in[1];
  const u16* wo = (const u16*)d_in[2];
  const u16* wg = (const u16*)d_in[4];
  const u16* wu = (const u16*)d_in[5];
  const u16* wd = (const u16*)d_in[6];
  char* ws = (char*)d_ws;
  u16* pq = (u16*)(ws);
  u16* h = (u16*)(ws + (4 << 20));
  float* kvp = (float*)(ws + (12 << 20));
  u16* wp = (u16*)(ws + (13 << 20));
  unsigned* flags = (unsigned*)(ws + (14 << 20));
  u16* wdt = (u16*)(ws + (16 << 20));
  (void)hipFuncSetAttribute((const void*)k1_kernel, hipFuncAttributeMaxDynamicSharedMemorySize, K1_LDS);
  (void)hipFuncSetAttribute((const void*)k2_kernel, hipFuncAttributeMaxDynamicSharedMemorySize, K2_LDS);
  k1_kernel<<<256, 512, K1_LDS, stream>>>(x, wqkv, wg, wu, pq, h, kvp, flags, wd, wo, wdt);
  k2_kernel<<<256, 512, K2_LDS, stream>>>(x, h, wdt, pq, kvp, wo, wp, flags, (u16*)d_out);
}
```
